```python
import torch

def _softmax(x, axis=-1):
    return torch.softmax(x, dim=axis)
def _gelu(x):
    return torch.nn.functional.gelu(x, approximate="tanh")
def _silu(x):
    return torch.nn.functional.silu(x)
def _sigmoid(x):
    return torch.sigmoid(x)
def _layernorm(x, axis=-1, eps=1e-5):
    m = x.mean(dim=axis, keepdim=True)
    v = x.var(dim=axis, keepdim=True, unbiased=False)
    return (x - m) / torch.sqrt(v + eps)
def _scatter_add(tgt, idx, src, axis=0):
    return tgt.clone().index_add_(axis, idx, src)
def _segment_sum(src, seg, n):
    out = torch.zeros((n,) + src.shape[1:], dtype=src.dtype)
    return out.index_add_(0, seg, src)
def _pad(x, pw):
    flat = tuple(v for pair in reversed(pw) for v in pair)
    return torch.nn.functional.pad(x, flat)

kernel_name = 'interleave_layers__sliding_local_dilated__glu_gated_reglu__prompt'

def setup_inputs():

    _w = (lambda a: (lambda t: t.to(torch.bfloat16) if t.is_floating_point() else t)(torch.as_tensor(a)))
    def _A_setup():
        B, S, D = 1, 2048, 576
        return {
            "x": _w((lambda *s: torch.randn(*s, dtype=torch.bfloat16) * 0.05)(B, S, D)),
            "wqkv": _w((lambda *s: torch.randn(*s, dtype=torch.bfloat16) * 0.05)(D, 3 * D)),
            "wo": _w((lambda *s: torch.randn(*s, dtype=torch.bfloat16) * 0.05)(D, D)),
        }
    _A_in = _A_setup()

    def _B_setup():
        B, S, D, F = 1, 2048, 576, 4608
        return {
            "x": _w((lambda *s: torch.randn(*s, dtype=torch.bfloat16) * 0.05)(B, S, D)),
            "W_gate": _w((lambda *s: torch.randn(*s, dtype=torch.bfloat16) * 0.8)(D, F) * (2.0 / D) ** 0.5),
            "W_up": _w((lambda *s: torch.randn(*s, dtype=torch.bfloat16) * 0.8)(D, F) * (2.0 / D) ** 0.5),
            "W_down": _w((lambda *s: torch.randn(*s, dtype=torch.bfloat16) * 0.8)(F, D) * (1.0 / F) ** 0.5),
        }
    _B_in = _B_setup()

    return {
        "A_x": _A_in["x"],
        "A_wqkv": _A_in["wqkv"],
        "A_wo": _A_in["wo"],
        "B_x": _B_in["x"],
        "B_W_gate": _B_in["W_gate"],
        "B_W_up": _B_in["W_up"],
        "B_W_down": _B_in["W_down"],
    }

def reference(A_x, A_wqkv, A_wo, B_x, B_W_gate, B_W_up, B_W_down):
    def _A_ref(x, wqkv, wo):
        B, S, D = x.shape
        H = 32; Dh = D // H
        W, dil = 8, 2
        qkv = (lambda x, *d: x.permute(*d))((x @ wqkv).reshape(B, S, 3, H, Dh), 2, 0, 3, 1, 4)
        q, k, v = qkv[0], qkv[1], qkv[2]
        scale = Dh ** -0.5
        pos = torch.arange(S)
        off = torch.arange(W) * dil
        idx = pos[:, None] - off[None, :]
        mask = idx >= 0
        idx_c = torch.where(mask, idx, 0).reshape(-1)
        kg = k[:, :, idx_c, :].reshape(B, H, S, W, Dh)
        vg = v[:, :, idx_c, :].reshape(B, H, S, W, Dh)
        logits = torch.einsum("bhsd,bhswd->bhsw", q, kg) * scale
        logits = torch.where(mask[None, None, :, :], logits, -1e9)
        attn = _softmax(logits, axis=-1)
        o = torch.einsum("bhsw,bhswd->bhsd", attn, vg)
        return (lambda x, *d: x.permute(*d))(o, 0, 2, 1, 3).reshape(B, S, D) @ wo

    def _B_ref(x, W_gate, W_up, W_down):
        gate = torch.einsum("bsd,df->bsf", x, W_gate)
        up = torch.einsum("bsd,df->bsf", x, W_up)
        gate = gate * (lambda x, dt: x.to(dt))(gate > 0, gate.dtype)
        h = gate * up
        return torch.einsum("bsf,fd->bsd", h, W_down)

    x = A_x
    x = x + _A_ref(_layernorm(x, axis=-1), A_wqkv, A_wo)
    x = x + _B_ref(_layernorm(x, axis=-1), B_W_gate, B_W_up, B_W_down)
    x = x + _A_ref(_layernorm(x, axis=-1), A_wqkv, A_wo)
    x = x + _B_ref(_layernorm(x, axis=-1), B_W_gate, B_W_up, B_W_down)
    return x
```

```cpp
#include <hip/hip_runtime.h>
#include <hip/hip_bf16.h>
#include <stdint.h>

#define GAS __attribute__((address_space(1)))
#define LAS __attribute__((address_space(3)))
typedef unsigned v4u __attribute__((ext_vector_type(4)));
typedef unsigned v2u __attribute__((ext_vector_type(2)));
typedef float f32x4 __attribute__((ext_vector_type(4)));
typedef float f32x2 __attribute__((ext_vector_type(2)));
typedef __bf16 bf16x8 __attribute__((ext_vector_type(8)));
typedef __bf16 bf16x2 __attribute__((ext_vector_type(2)));
typedef unsigned short u16;

constexpr int S_ = 2048, D_ = 576, F_ = 4608, H_ = 32, DH = 18;
constexpr int KS = 18;
constexpr size_t XF_BYTES = (size_t)S_ * D_ * 2;
constexpr int TT_BYTES = KS * 1024;

constexpr size_t OFF_WQ = 0;
constexpr size_t OFF_WO = OFF_WQ + (size_t)32 * 72 * 1024;
constexpr size_t OFF_WF = OFF_WO + (size_t)36 * 18 * 1024;
constexpr size_t OFF_CSQ = OFF_WF + (size_t)64 * 243 * 1024;
constexpr size_t OFF_CSG = OFF_CSQ + 32 * 64 * 4;
constexpr size_t OFF_ST = OFF_CSG + 64 * 9 * 16 * 4;
constexpr size_t OFF_PM = OFF_ST + 2048 * 8;
constexpr size_t OFF_PV = OFF_PM + 2048 * 16;
constexpr size_t OFF_X0 = OFF_PV + 2048 * 16;
constexpr size_t OFF_X1 = OFF_X0 + XF_BYTES;
constexpr size_t OFF_O = OFF_X1 + XF_BYTES;
constexpr size_t OFF_P = OFF_O + XF_BYTES;
constexpr size_t WS_TOTAL = OFF_P + 16 * XF_BYTES;

#define MIMIC 1

__device__ __forceinline__ f32x4 mfma(v4u a, v4u b, f32x4 c) {
  return __builtin_amdgcn_mfma_f32_16x16x32_bf16(__builtin_bit_cast(bf16x8, a), __builtin_bit_cast(bf16x8, b), c, 0, 0, 0);
}
__device__ __forceinline__ unsigned pack2(float a, float b) {
  f32x2 v = {a, b};
  return __builtin_bit_cast(unsigned, __builtin_convertvector(v, bf16x2));
}
__device__ __forceinline__ float bflo(unsigned u) { return __builtin_bit_cast(float, u << 16); }
__device__ __forceinline__ float bfhi(unsigned u) { return __builtin_bit_cast(float, u & 0xffff0000u); }
__device__ __forceinline__ float rbf(float a) {
  return bflo(pack2(a, 0.f));
}
__device__ __forceinline__ float ln_mean(float m) {
  return rbf(m);
}
__device__ __forceinline__ float ln_rstd(float m2) {
  float v = m2 * (1.0f / D_);
  return 1.0f / rbf(sqrtf(rbf(rbf(v) + 1e-5f)));
}
__device__ __forceinline__ void glds16(const void* g, LAS void* l) {
  __builtin_amdgcn_global_load_lds((const GAS void*)g, l, 16, 0, 0);
}

#define SB() __builtin_amdgcn_sched_barrier(0)
#define STAMP(i)
#define STAMP_DECL
#define STAMP_OUT(k)
__device__ __forceinline__ void mfma_vvv(f32x4& c, v4u a, v4u b) {
  asm volatile("v_mfma_f32_16x16x32_bf16 %0, %1, %2, %0" : "+v"(c) : "v"(a), "v"(b));
}
__device__ __forceinline__ void mfma_avv(f32x4& c, v4u a, v4u b) {
  asm volatile("v_mfma_f32_16x16x32_bf16 %0, %1, %2, %0" : "+a"(c) : "v"(a), "v"(b));
}
template <bool FIRST, bool BA>
__device__ __forceinline__ void mfma_2(f32x4& c, v4u a, v4u b) {
  if (FIRST) {
    if (BA) asm volatile("v_mfma_f32_16x16x32_bf16 %0, %1, %2, 0" : "=&v"(c) : "v"(a), "a"(b));
    else asm volatile("v_mfma_f32_16x16x32_bf16 %0, %1, %2, 0" : "=&v"(c) : "v"(a), "v"(b));
  } else {
    if (BA) asm volatile("v_mfma_f32_16x16x32_bf16 %0, %1, %2, %0" : "+v"(c) : "v"(a), "a"(b));
    else asm volatile("v_mfma_f32_16x16x32_bf16 %0, %1, %2, %0" : "+v"(c) : "v"(a), "v"(b));
  }
}
__device__ __forceinline__ float relu(float x) {
  float r;
  asm("v_max_f32 %0, 0, %1" : "=v"(r) : "v"(x));
  return r;
}
__global__ __launch_bounds__(256) void k_prep(const u16* __restrict__ x, const u16* __restrict__ wqkv, const u16* __restrict__ wo,
                                              const u16* __restrict__ wg, const u16* __restrict__ wu, const u16* __restrict__ wd,
                                              unsigned char* __restrict__ ws) {
  __shared__ float red[4][16];
  __shared__ float red2[16];
  const int tid = threadIdx.x, lane = tid & 63, w = tid >> 6, r = lane & 15, q = lane >> 4;
  int u = blockIdx.x;
  if (u < 576) {
    const int run = u / 9, a = u % 9;
    const int f = 72 * run + 8 * a + 2 * (r >> 2) + (r & 1);
    const u16* src = ((r & 2) ? wu : wg) + f;
    float cs = 0.f;
    for (int s = w; s < KS; s += 4) {
      unsigned e[8];
#pragma unroll
      for (int j = 0; j < 8; ++j) e[j] = src[(size_t)(32 * s + 8 * q + j) * F_];
      v4u o;
#pragma unroll
      for (int j = 0; j < 4; ++j) { o[j] = e[2 * j] | (e[2 * j + 1] << 16); cs += bflo(e[2 * j]) + bflo(e[2 * j + 1]); }
      *(v4u*)(ws + OFF_WF + ((size_t)run * 243 + s * 9 + a) * 1024 + lane * 16) = o;
    }
    cs += __shfl_xor(cs, 16); cs += __shfl_xor(cs, 32);
    if (lane < 16) red[w][lane] = cs;
    __syncthreads();
    if (tid < 16) ((float*)(ws + OFF_CSG))[u * 16 + tid] = red[0][tid] + red[1][tid] + red[2][tid] + red[3][tid];
    return;
  }
  u -= 576;
  if (u < 576) {
    const int run = u / 9, a = u % 9, j16 = run >> 2, wv = run & 3;
    const int T = (wv & 1) ? (a < 8 ? 9 * wv + 1 + a : 9 * wv) : 9 * wv + a;
    const int n = 32 * (T >> 1) + 8 * (r >> 2) + 4 * (T & 1) + (r & 3);
    for (int s = w; s < 9; s += 4) {
      unsigned e[8];
#pragma unroll
      for (int j = 0; j < 8; ++j) {
        int ww, aa;
        if (s < 8) { ww = s >> 1; aa = 4 * (s & 1) + (j >> 1); } else { ww = j >> 1; aa = 8; }
        const int f = 288 * j16 + 72 * ww + 8 * aa + 2 * q + (j & 1);
        e[j] = wd[(size_t)f * D_ + n];
      }
      v4u o;
#pragma unroll
      for (int j = 0; j < 4; ++j) o[j] = e[2 * j] | (e[2 * j + 1] << 16);
      *(v4u*)(ws + OFF_WF + ((size_t)run * 243 + 162 + (a < 8 ? (a >> 1) * 18 + s * 2 + (a & 1) : 72 + s)) * 1024 + lane * 16) = o;
    }
    return;
  }
  u -= 576;
  if (u < 128) {
    const int h = u >> 2, a = u & 3, rho = 16 * a + r;
    int col = rho < 18 ? 576 + 18 * h + rho : rho < 36 ? 1152 + 18 * h + rho - 18 : 18 * h + rho - 36;
    const bool ok = rho < 54;
    if (!ok) col = 0;
    float cs = 0.f;
    for (int s = w; s < KS; s += 4) {
      unsigned e[8];
#pragma unroll
      for (int j = 0; j < 8; ++j) { unsigned t = wqkv[(size_t)(32 * s + 8 * q + j) * 1728 + col]; e[j] = ok ? t : 0u; }
      v4u o;
#pragma unroll
      for (int j = 0; j < 4; ++j) { o[j] = e[2 * j] | (e[2 * j + 1] << 16); cs += bflo(e[2 * j]) + bflo(e[2 * j + 1]); }
      *(v4u*)(ws + OFF_WQ + ((size_t)h * 72 + s * 4 + a) * 1024 + lane * 16) = o;
    }
    cs += __shfl_xor(cs, 16); cs += __shfl_xor(cs, 32);
    if (lane < 16) red[w][lane] = cs;
    __syncthreads();
    if (tid < 16) ((float*)(ws + OFF_CSQ))[u * 16 + tid] = red[0][tid] + red[1][tid] + red[2][tid] + red[3][tid];
    return;
  }
  u -= 128;
  if (u < 36) {
    const int T = u, n = 16 * T + r;
    for (int s = w; s < KS; s += 4) {
      const int g = 4 * s + q, hg = g / 9, i = g % 9;
      unsigned e[8];
#pragma unroll
      for (int j = 0; j < 8; ++j) {
        const int c = i < 8 ? (4 * hg + (i >> 1)) * 18 + (i & 1) * 8 + j : (4 * hg + (j >> 1)) * 18 + 16 + (j & 1);
        e[j] = wo[(size_t)c * D_ + n];
      }
      v4u o;
#pragma unroll
      for (int j = 0; j < 4; ++j) o[j] = e[2 * j] | (e[2 * j + 1] << 16);
      *(v4u*)(ws + OFF_WO + ((size_t)((T / 3) * 18 + s) * 3 + T % 3) * 1024 + lane * 16) = o;
    }
    return;
  }
  u -= 36;
  {
    v4u v[5];
    float sum = 0.f;
#pragma unroll
    for (int i = 0; i < 5; ++i) {
      const int s = w + 4 * i;
      if (s < KS) {
        v[i] = *(const v4u*)(x + (size_t)(16 * u + r) * D_ + 32 * s + 8 * q);
        *(v4u*)(ws + OFF_X0 + (size_t)u * TT_BYTES + s * 1024 + lane * 16) = v[i];
#pragma unroll
        for (int j = 0; j < 4; ++j) sum += bflo(v[i][j]) + bfhi(v[i][j]);
      }
    }
    sum += __shfl_xor(sum, 16); sum += __shfl_xor(sum, 32);
    if (lane < 16) red[w][lane] = sum;
    __syncthreads();
    const float mean = (red[0][r] + red[1][r] + red[2][r] + red[3][r]) * (1.0f / D_);
    float m2 = 0.f;
#pragma unroll
    for (int i = 0; i < 5; ++i) {
      const int s = w + 4 * i;
      if (s < KS) {
#pragma unroll
        for (int j = 0; j < 4; ++j) { float a = bflo(v[i][j]) - mean, b = bfhi(v[i][j]) - mean; m2 += a * a + b * b; }
      }
    }
    m2 += __shfl_xor(m2, 16); m2 += __shfl_xor(m2, 32);
    __syncthreads();
    if (lane < 16) red[w][lane] = m2;
    if (tid < 16) red2[tid] = mean;
    __syncthreads();
    if (tid < 16) {
      const float t = red[0][tid] + red[1][tid] + red[2][tid] + red[3][tid];
      f32x2 o = {ln_mean(red2[tid]), ln_rstd(t)};
      ((f32x2*)(ws + OFF_ST))[16 * u + tid] = o;
    }
  }
}

constexpr int QS = 88;
constexpr int K1_LDS = 90 * 1024 + 4 * 27 * QS * 4;
__global__ __launch_bounds__(256, 1) void k_qkv_attn(unsigned char* __restrict__ ws) {
  extern __shared__ __attribute__((aligned(16))) unsigned char smem_[];
  LAS unsigned char* L = (LAS unsigned char*)smem_;
  const int tid = threadIdx.x, lane = tid & 63, w = __builtin_amdgcn_readfirstlane(tid >> 6), tok = lane & 15, q = lane >> 4;
  const int b = blockIdx.x, hg = b & 7, mt = b >> 3;
  const int h = 4 * hg + w;
  const int tt0 = 4 * mt - 1;
  for (int i = w; i < 90; i += 4) {
    const int s = i / 5, t = i % 5;
    int tt = tt0 + t; tt = tt < 0 ? 0 : tt;
    glds16(ws + OFF_X0 + (size_t)tt * TT_BYTES + s * 1024 + lane * 16, L + i * 1024);
  }
  const GAS v4u* wp = (const GAS v4u*)(ws + OFF_WQ + (size_t)h * 72 * 1024) + lane;
  constexpr int WIN = 36;
  v4u ring[WIN];
#pragma unroll
  for (int i = 0; i < WIN; ++i) ring[i] = wp[i * 64];
  SB();
  float mean[5], rstd[5];
#pragma unroll
  for (int t = 0; t < 5; ++t) {
    int tt = tt0 + t; tt = tt < 0 ? 0 : tt;
    const f32x2 st = ((const GAS f32x2*)(ws + OFF_ST))[16 * tt + tok];
    mean[t] = st[0]; rstd[t] = st[1];
  }
  f32x4 acc[4][5];
#pragma unroll
  for (int a = 0; a < 4; ++a) {
    const f32x4 cs = *(const GAS f32x4*)(ws + OFF_CSQ + (size_t)(h * 64 + a * 16 + 4 * q) * 4);
#pragma unroll
    for (int t = 0; t < 5; ++t) { acc[a][t] = cs * (-mean[t]); asm volatile("" : "+v"(acc[a][t])); }
  }
  asm volatile("s_waitcnt vmcnt(0)" ::: "memory");
  __syncthreads();
#pragma unroll
  for (int s = 0; s < KS; ++s) {
    v4u B[5];
#pragma unroll
    for (int t = 0; t < 5; ++t) B[t] = *(LAS v4u*)(L + (s * 5 + t) * 1024 + lane * 16);
#pragma unroll
    for (int a = 0; a < 4; ++a) {
      const int i = s * 4 + a;
#pragma unroll
      for (int t = 0; t < 5; ++t)
        if (!(a == 3 && t == 0)) mfma_vvv(acc[a][t], ring[i % WIN], B[t]);
      if (i + WIN < 72) ring[i % WIN] = wp[(i + WIN) * 64];
    }
  }
  SB();
  asm volatile("s_nop 7" : "+v"(acc[2][0]), "+v"(acc[2][1]), "+v"(acc[2][2]), "+v"(acc[2][3]), "+v"(acc[2][4]), "+v"(acc[3][1]), "+v"(acc[3][2]), "+v"(acc[3][3]), "+v"(acc[3][4]));
  SB();
  LAS unsigned* E = (LAS unsigned*)(L + 90 * 1024) + w * 27 * QS;
#pragma unroll
  for (int a = 0; a < 4; ++a)
#pragma unroll
    for (int t = 0; t < 5; ++t) {
      if (a == 3 && t == 0) continue;
      const f32x4 v = acc[a][t] * rstd[t];
      const int p = 8 * a + 2 * q;
      if (p < 27) E[p * QS + 16 * t + tok] = pack2(v[0], v[1]);
      if (p + 1 < 27) E[(p + 1) * QS + 16 * t + tok] = pack2(v[2], v[3]);
    }
  __builtin_amdgcn_fence(__ATOMIC_RELEASE, "wavefront");
  __builtin_amdgcn_wave_barrier();
  __builtin_amdgcn_fence(__ATOMIC_ACQUIRE, "wavefront");
  const int slot = 16 + lane, pos = 64 * mt + lane;
  unsigned qv[9];
#pragma unroll
  for (int p = 0; p < 9; ++p) qv[p] = E[(18 + p) * QS + slot];
  float lg[8];
  const float sc = 0.23570226039551584f * 1.4426950408889634f;
#pragma unroll
  for (int i = 0; i < 8; ++i) {
    float d = 0.f;
#pragma unroll
    for (int p = 0; p < 9; ++p) {
      const unsigned kv = E[p * QS + slot - 2 * i];
      d = __builtin_amdgcn_fdot2_f32_bf16(__builtin_bit_cast(bf16x2, qv[p]), __builtin_bit_cast(bf16x2, kv), d, false);
    }
    lg[i] = (pos - 2 * i >= 0) ? d * sc : -1e30f;
  }
  float mx = lg[0];
#pragma unroll
  for (int i = 1; i < 8; ++i) mx = fmaxf(mx, lg[i]);
  float sum = 0.f;
#pragma unroll
  for (int i = 0; i < 8; ++i) { lg[i] = __builtin_amdgcn_exp2f(lg[i] - mx); sum += lg[i]; }
  const float inv = 1.0f / sum;
  float o[18];
#pragma unroll
  for (int d = 0; d < 18; ++d) o[d] = 0.f;
#pragma unroll
  for (int i = 0; i < 8; ++i) {
    const float pw = lg[i] * inv;
#pragma unroll
    for (int p = 0; p < 9; ++p) {
      const unsigned vv = E[(9 + p) * QS + slot - 2 * i];
      o[2 * p] += pw * bflo(vv);
      o[2 * p + 1] += pw * bfhi(vv);
    }
  }
  unsigned char* ob = ws + OFF_O + (size_t)(4 * mt + q) * TT_BYTES + tok * 16;
  v4u p0 = {pack2(o[0], o[1]), pack2(o[2], o[3]), pack2(o[4], o[5]), pack2(o[6], o[7])};
  v4u p1 = {pack2(o[8], o[9]), pack2(o[10], o[11]), pack2(o[12], o[13]), pack2(o[14], o[15])};
  *(v4u*)(ob + (9 * hg + 2 * w) * 256) = p0;
  *(v4u*)(ob + (9 * hg + 2 * w + 1) * 256) = p1;
  *(unsigned*)(ob + (9 * hg + 8) * 256 + 4 * w) = pack2(o[16], o[17]);
}

__global__ __launch_bounds__(192, 1) void k_wo(unsigned char* __restrict__ ws) {
  __shared__ float red[3][32];
  __shared__ float red2[3][32];
  const int tid = threadIdx.x, lane = tid & 63, w = __builtin_amdgcn_readfirstlane(tid >> 6), tok = lane & 15, q = lane >> 4;
  const int b = blockIdx.x, nb = b & 3, mt = b >> 2;
  const GAS v4u* wp = (const GAS v4u*)(ws + OFF_WO + (size_t)(3 * nb + w) * 54 * 1024) + lane;
  const GAS v4u* bp = (const GAS v4u*)(ws + OFF_O + (size_t)(2 * mt) * TT_BYTES) + lane;
  f32x4 acc[3][2];
  v4u A[54], B[36];
#pragma unroll
  for (int s = 0; s < KS; ++s) {
    B[2 * s] = bp[s * 64]; B[2 * s + 1] = bp[(KS + s) * 64];
#pragma unroll
    for (int a = 0; a < 3; ++a) A[3 * s + a] = wp[(3 * s + a) * 64];
  }
  v2u res[3][2];
#pragma unroll
  for (int a = 0; a < 3; ++a)
#pragma unroll
    for (int t = 0; t < 2; ++t) {
      const int T = 9 * nb + 3 * w + a;
      res[a][t] = *(const GAS v2u*)(ws + OFF_X0 + (size_t)(2 * mt + t) * TT_BYTES + T * 512 + ((q >> 1) * 16 + tok) * 16 + (q & 1) * 8);
    }
  __builtin_amdgcn_sched_barrier(0);
#pragma unroll
  for (int s = 0; s < KS; ++s)
#pragma unroll
    for (int a = 0; a < 3; ++a) {
      if (s == 0) { mfma_2<true, true>(acc[a][0], A[3 * s + a], B[2 * s]); mfma_2<true, true>(acc[a][1], A[3 * s + a], B[2 * s + 1]); }
      else { mfma_2<false, true>(acc[a][0], A[3 * s + a], B[2 * s]); mfma_2<false, true>(acc[a][1], A[3 * s + a], B[2 * s + 1]); }
    }
  asm volatile("s_nop 7" : "+v"(acc[0][0]), "+v"(acc[0][1]), "+v"(acc[1][0]), "+v"(acc[1][1]), "+v"(acc[2][0]), "+v"(acc[2][1]));
  float xv[2][12];
  float sum[2] = {0.f, 0.f};
#pragma unroll
  for (int a = 0; a < 3; ++a)
#pragma unroll
    for (int t = 0; t < 2; ++t) {
      const int T = 9 * nb + 3 * w + a;
      f32x4 y = acc[a][t];
      y[0] = rbf(y[0]); y[1] = rbf(y[1]); y[2] = rbf(y[2]); y[3] = rbf(y[3]);
      v2u o;
      o[0] = pack2(bflo(res[a][t][0]) + y[0], bfhi(res[a][t][0]) + y[1]);
      o[1] = pack2(bflo(res[a][t][1]) + y[2], bfhi(res[a][t][1]) + y[3]);
      *(v2u*)(ws + OFF_X1 + (size_t)(2 * mt + t) * TT_BYTES + T * 512 + ((q >> 1) * 16 + tok) * 16 + (q & 1) * 8) = o;
      xv[t][4 * a] = bflo(o[0]); xv[t][4 * a + 1] = bfhi(o[0]); xv[t][4 * a + 2] = bflo(o[1]); xv[t][4 * a + 3] = bfhi(o[1]);
      sum[t] += (xv[t][4 * a] + xv[t][4 * a + 1]) + (xv[t][4 * a + 2] + xv[t][4 * a + 3]);
    }
#pragma unroll
  for (int t = 0; t < 2; ++t) {
    sum[t] += __shfl_xor(sum[t], 16); sum[t] += __shfl_xor(sum[t], 32);
    if (lane < 16) red[w][16 * t + lane] = sum[t];
  }
  __syncthreads();
  float mean[2];
#pragma unroll
  for (int t = 0; t < 2; ++t) {
    mean[t] = (red[0][16 * t + tok] + red[1][16 * t + tok] + red[2][16 * t + tok]) * (1.0f / 144);
    float m2 = 0.f;
#pragma unroll
    for (int i = 0; i < 12; ++i) { const float d = xv[t][i] - mean[t]; m2 += d * d; }
    m2 += __shfl_xor(m2, 16); m2 += __shfl_xor(m2, 32);
    if (lane < 16) red2[w][16 * t + lane] = m2;
  }
  __syncthreads();
  if (tid < 32) {
    const int token = 32 * mt + tid;
    ((float*)(ws + OFF_PM))[token * 4 + nb] = (red[0][tid] + red[1][tid] + red[2][tid]) * (1.0f / 144);
    ((float*)(ws + OFF_PV))[token * 4 + nb] = red2[0][tid] + red2[1][tid] + red2[2][tid];
  }
}

constexpr int FFN_LDS = 152 * 1024;
template <int R0>
__device__ __forceinline__ void kstep(f32x4 (&acc)[9][8], v4u (&ring)[18], const v4u (&B)[8], const GAS v4u* wp) {
#pragma unroll
  for (int a = 0; a < 9; ++a) {
#pragma unroll
    for (int t = 0; t < 8; ++t) {
      if (a == 0) mfma_vvv(acc[0][t], ring[R0], B[t]);
      else mfma_avv(acc[a][t], ring[R0 + a], B[t]);
    }
    ring[R0 + a] = wp[(R0 + a) * 64];
    SB();
  }
}
__device__ __forceinline__ void ldB(v4u (&B)[8], LAS unsigned char* lp) {
#pragma unroll
  for (int t = 0; t < 8; ++t) B[t] = *(LAS v4u*)(lp + t * 1024);
  SB();
}
__global__ __launch_bounds__(256, 1) void k_ffn(unsigned char* __restrict__ ws) {
  extern __shared__ __attribute__((aligned(16))) unsigned char smem_[];
  LAS unsigned char* L = (LAS unsigned char*)smem_;
  const int tid = threadIdx.x, lane = tid & 63, w = __builtin_amdgcn_readfirstlane(tid >> 6), tok = lane & 15, q = lane >> 4;
  const int b = blockIdx.x, j16 = (b & 7) * 2 + ((b >> 3) & 1), m = b >> 4;
  STAMP_DECL;
  STAMP(0);
  for (int i = w; i < 144; i += 4) {
    const int s = i >> 3, t = i & 7;
    glds16(ws + OFF_X1 + (size_t)(8 * m + t) * TT_BYTES + s * 1024 + lane * 16, L + i * 1024);
  }
  const int run = 4 * j16 + w;
  const GAS v4u* wp = (const GAS v4u*)(ws + OFF_WF + (size_t)run * 243 * 1024) + lane;
  v4u ring[18];
#pragma unroll
  for (int i = 0; i < 18; ++i) ring[i] = wp[i * 64];
  f32x4 acc[9][8];
  {
    float mean[8];
#pragma unroll
    for (int t = 0; t < 8; ++t) {
      const f32x4 pm = ((const GAS f32x4*)(ws + OFF_PM))[128 * m + 16 * t + tok];
      mean[t] = -ln_mean(((pm[0] + pm[1]) + (pm[2] + pm[3])) * 0.25f);
    }
#pragma unroll
    for (int a = 0; a < 9; ++a) {
      const f32x4 cs = *(const GAS f32x4*)(ws + OFF_CSG + (size_t)((run * 9 + a) * 16 + 4 * q) * 4);
#pragma unroll
      for (int t = 0; t < 8; ++t) {
        acc[a][t] = cs * mean[t];
        if (a == 0) asm volatile("" : "+v"(acc[a][t])); else asm volatile("" : "+a"(acc[a][t]));
      }
      SB();
    }
  }
  STAMP(1);
  asm volatile("s_waitcnt vmcnt(0)" ::: "memory");
  __syncthreads();
  STAMP(2);
  LAS unsigned char* lp = L + lane * 16;
  {
    v4u B0[8], B1[8];
    ldB(B0, lp);
    wp += 18 * 64;
#pragma unroll 1
    for (int it = 0; it < 9; ++it) {
      ldB(B1, lp + 8192);
      kstep<0>(acc, ring, B0, wp);
      lp += 16 * 1024;
      ldB(B0, lp);
      kstep<9>(acc, ring, B1, wp);
      wp += 18 * 64;
    }
  }
  asm volatile("s_nop 7" : "+a"(acc[8][0]), "+a"(acc[8][1]), "+a"(acc[8][2]), "+a"(acc[8][3]), "+a"(acc[8][4]), "+a"(acc[8][5]), "+a"(acc[8][6]), "+a"(acc[8][7]));
  SB();
  STAMP(3);
  unsigned hreg[9][8];
#pragma unroll
  for (int a = 0; a < 9; ++a) {
#pragma unroll
    for (int t = 0; t < 8; ++t) {
      const f32x4 v = acc[a][t];
      hreg[a][t] = pack2(relu(v[0]) * v[2], relu(v[1]) * v[3]);
    }
    SB();
  }
  STAMP(4);
  __syncthreads();
  lp = L + lane * 16;
#pragma unroll
  for (int t = 0; t < 8; ++t) {
    *(LAS v4u*)(lp + ((2 * w) * 8 + t) * 1024) = v4u{hreg[0][t], hreg[1][t], hreg[2][t], hreg[3][t]};
    *(LAS v4u*)(lp + ((2 * w + 1) * 8 + t) * 1024) = v4u{hreg[4][t], hreg[5][t], hreg[6][t], hreg[7][t]};
    *(LAS unsigned*)(lp + (64 + t) * 1024 + 4 * w) = hreg[8][t];
  }
  __syncthreads();
  v4u Hf[9][8];
#pragma unroll
  for (int s = 0; s < 9; ++s)
#pragma unroll
    for (int t = 0; t < 8; ++t) {
      Hf[s][t] = *(LAS v4u*)(lp + (s * 8 + t) * 1024);
    }
  SB();
  STAMP(5);
  unsigned char* pb = ws + OFF_P + (size_t)j16 * XF_BYTES + (size_t)(8 * m) * TT_BYTES + lane * 16;
  const int s0 = (9 * w + (w & 1)) >> 1;
  const int T8 = (w & 1) ? 9 * w : 9 * w + 8;
  unsigned char* pp = pb + s0 * 1024;
#pragma unroll 1
  for (int p = 0; p < 4; ++p) {
    f32x4 c0[8], c1[8];
#pragma unroll
    for (int s = 0; s < 8; ++s) {
#pragma unroll
      for (int t = 0; t < 8; ++t) { if (s == 0) mfma_2<true, false>(c0[t], ring[2 * s], Hf[s][t]); else mfma_2<false, true>(c0[t], ring[2 * s], Hf[s][t]); }
      ring[2 * s] = wp[(2 * s) * 64];
      SB();
#pragma unroll
      for (int t = 0; t < 8; ++t) { if (s == 0) mfma_2<true, false>(c1[t], ring[2 * s + 1], Hf[s][t]); else mfma_2<false, true>(c1[t], ring[2 * s + 1], Hf[s][t]); }
      ring[2 * s + 1] = wp[(2 * s + 1) * 64];
      SB();
    }
#pragma unroll
    for (int t = 0; t < 12; ++t) {
      if (t < 8) {
        mfma_2<false, true>(c0[t], ring[16], Hf[8][t]);
        mfma_2<false, true>(c1[t], ring[17], Hf[8][t]);
      }
      if (t == 8) {
        ring[16] = wp[16 * 64];
        ring[17] = wp[17 * 64];
        asm volatile("s_nop 7");
      }
      SB();
      if (t >= 4) {
        const f32x4 u0 = c0[t - 4], u1 = c1[t - 4];
        *(v4u*)(pp + (size_t)(t - 4) * TT_BYTES) = v4u{pack2(u0[0], u0[1]), pack2(u0[2], u0[3]), pack2(u1[0], u1[1]), pack2(u1[2], u1[3])};
      }
      SB();
    }
    wp += 18 * 64;
    pp += 1024;
  }
  {
    f32x4 c0[8];
#pragma unroll
    for (int s = 0; s < 9; ++s) {
#pragma unroll
      for (int t = 0; t < 8; ++t) { if (s == 0) mfma_2<true, false>(c0[t], ring[s], Hf[s][t]); else mfma_2<false, true>(c0[t], ring[s], Hf[s][t]); }
      SB();
    }
    asm volatile("s_nop 7");
    SB();
#pragma unroll
    for (int t = 0; t < 8; ++t) {
      const f32x4 u8 = c0[t];
      *(v2u*)(pb + (size_t)t * TT_BYTES + (T8 >> 1) * 1024 + (T8 & 1) * 8) = v2u{pack2(u8[0], u8[1]), pack2(u8[2], u8[3])};
    }
  }
  STAMP(6);
  STAMP(7);
  STAMP_OUT(0);
}

template <bool LAST>
__global__ __launch_bounds__(576, 1) void k_reduce(unsigned char* __restrict__ ws, u16* __restrict__ out) {
  __shared__ float red[9][8];
  __shared__ float red2[9][8];
  const int tid = threadIdx.x, lane = tid & 63, w = tid >> 6, tk = tid & 7, g = tid >> 3;
  const int b = blockIdx.x, tt = b >> 1, token = 8 * b + tk;
  const size_t off = (size_t)tt * TT_BYTES + g * 256 + (8 * (b & 1) + tk) * 16;
  v4u pv[16];
#pragma unroll
  for (int j = 0; j < 16; ++j) pv[j] = *(const GAS v4u*)(ws + OFF_P + (size_t)j * XF_BYTES + off);
  const v4u xr = *(const GAS v4u*)(ws + OFF_X1 + off);
  const f32x4 pm = ((const GAS f32x4*)(ws + OFF_PM))[token];
  const f32x4 p2 = ((const GAS f32x4*)(ws + OFF_PV))[token];
  const float mu = ((pm[0] + pm[1]) + (pm[2] + pm[3])) * 0.25f;
  float m2 = (p2[0] + p2[1]) + (p2[2] + p2[3]);
#pragma unroll
  for (int i = 0; i < 4; ++i) m2 += 144.f * (pm[i] - mu) * (pm[i] - mu);
  const float r = ln_rstd(m2), r2 = r * r;
  float acc[8];
#pragma unroll
  for (int i = 0; i < 8; ++i) acc[i] = 0.f;
#pragma unroll
  for (int j = 0; j < 16; ++j)
#pragma unroll
    for (int i = 0; i < 4; ++i) { acc[2 * i] += bflo(pv[j][i]); acc[2 * i + 1] += bfhi(pv[j][i]); }
  v4u o;
  float sum = 0.f;
#pragma unroll
  for (int i = 0; i < 4; ++i) {
    float y0 = acc[2 * i] * r2, y1 = acc[2 * i + 1] * r2;
    y0 = rbf(y0); y1 = rbf(y1);
    o[i] = pack2(bflo(xr[i]) + y0, bfhi(xr[i]) + y1);
    sum += bflo(o[i]) + bfhi(o[i]);
  }
  if (LAST) {
    *(v4u*)(out + (size_t)token * D_ + g * 8) = o;
    return;
  }
  *(v4u*)(ws + OFF_X0 + off) = o;
  sum += __shfl_xor(sum, 8); sum += __shfl_xor(sum, 16); sum += __shfl_xor(sum, 32);
  if (lane < 8) red[w][lane] = sum;
  __syncthreads();
  float mean = 0.f;
#pragma unroll
  for (int i = 0; i < 9; ++i) mean += red[i][tk];
  mean *= (1.0f / D_);
  float q2 = 0.f;
#pragma unroll
  for (int i = 0; i < 4; ++i) { const float a = bflo(o[i]) - mean, c = bfhi(o[i]) - mean; q2 += a * a + c * c; }
  q2 += __shfl_xor(q2, 8); q2 += __shfl_xor(q2, 16); q2 += __shfl_xor(q2, 32);
  if (lane < 8) red2[w][lane] = q2;
  __syncthreads();
  if (tid < 8) {
    float t = 0.f;
#pragma unroll
    for (int i = 0; i < 9; ++i) t += red2[i][tid];
    f32x2 st = {ln_mean(mean), ln_rstd(t)};
    ((f32x2*)(ws + OFF_ST))[token] = st;
  }
}

extern "C" { int g_stop = 1000; size_t g_offs[16] = {OFF_WQ, OFF_WO, OFF_WF, OFF_CSQ, OFF_CSG, OFF_ST, OFF_PM, OFF_PV, OFF_X0, OFF_X1, OFF_O, OFF_P, WS_TOTAL}; }
extern "C" void kernel_launch(void* const* d_in, const int* in_sizes, int n_in, void* d_out, int out_size, void* d_ws,
                              size_t ws_size, hipStream_t stream) {
  const u16* x = (const u16*)d_in[0];
  const u16* wqkv = (const u16*)d_in[1];
  const u16* wo = (const u16*)d_in[2];
  const u16* wg = (const u16*)d_in[4];
  const u16* wu = (const u16*)d_in[5];
  const u16* wd = (const u16*)d_in[6];
  unsigned char* ws = (unsigned char*)d_ws;
  (void)hipFuncSetAttribute((const void*)k_qkv_attn, hipFuncAttributeMaxDynamicSharedMemorySize, K1_LDS);
  (void)hipFuncSetAttribute((const void*)k_ffn, hipFuncAttributeMaxDynamicSharedMemorySize, FFN_LDS);
  int nk = 0;
#define STOPCHK() if (++nk >= g_stop) return
  k_prep<<<576 + 576 + 128 + 36 + 128, 256, 0, stream>>>(x, wqkv, wo, wg, wu, wd, ws);
  STOPCHK();
  for (int l = 0; l < 2; ++l) {
    k_qkv_attn<<<256, 256, K1_LDS, stream>>>(ws);
    STOPCHK();
    k_wo<<<256, 192, 0, stream>>>(ws);
    STOPCHK();
    k_ffn<<<256, 256, FFN_LDS, stream>>>(ws);
    STOPCHK();
    if (l == 0) k_reduce<false><<<256, 576, 0, stream>>>(ws, (u16*)d_out);
    else k_reduce<true><<<256, 576, 0, stream>>>(ws, (u16*)d_out);
    STOPCHK();
  }
}
```

```cpp
#include <hip/hip_runtime.h>
#include <hip/hip_bf16.h>
#include <stdint.h>

#define GAS __attribute__((address_space(1)))
#define LAS __attribute__((address_space(3)))
typedef unsigned v4u __attribute__((ext_vector_type(4)));
typedef unsigned v2u __attribute__((ext_vector_type(2)));
typedef float f32x4 __attribute__((ext_vector_type(4)));
typedef float f32x2 __attribute__((ext_vector_type(2)));
typedef __bf16 bf16x8 __attribute__((ext_vector_type(8)));
typedef __bf16 bf16x2 __attribute__((ext_vector_type(2)));
typedef unsigned short u16;

constexpr int S_ = 2048, D_ = 576, F_ = 4608, H_ = 32, DH = 18;
constexpr int KS = 18;
constexpr size_t XF_BYTES = (size_t)S_ * D_ * 2;
constexpr int TT_BYTES = KS * 1024;

constexpr size_t OFF_WQ = 0;
constexpr size_t OFF_WO = OFF_WQ + (size_t)32 * 72 * 1024;
constexpr size_t OFF_WF = OFF_WO + (size_t)36 * 18 * 1024;
constexpr size_t OFF_CSQ = OFF_WF + (size_t)64 * 243 * 1024;
constexpr size_t OFF_CSG = OFF_CSQ + 32 * 64 * 4;
constexpr size_t OFF_ST = OFF_CSG + 64 * 9 * 16 * 4;
constexpr size_t OFF_PM = OFF_ST + 2048 * 8;
constexpr size_t OFF_PV = OFF_PM + 2048 * 16;
constexpr size_t OFF_X0 = OFF_PV + 2048 * 16;
constexpr size_t OFF_X1 = OFF_X0 + XF_BYTES;
constexpr size_t OFF_O = OFF_X1 + XF_BYTES;
constexpr size_t OFF_P = OFF_O + XF_BYTES;
constexpr size_t WS_TOTAL = OFF_P + 16 * XF_BYTES;

#define MIMIC 0

__device__ __forceinline__ f32x4 mfma(v4u a, v4u b, f32x4 c) {
  return __builtin_amdgcn_mfma_f32_16x16x32_bf16(__builtin_bit_cast(bf16x8, a), __builtin_bit_cast(bf16x8, b), c, 0, 0, 0);
}
__device__ __forceinline__ unsigned pack2(float a, float b) {
  f32x2 v = {a, b};
  return __builtin_bit_cast(unsigned, __builtin_convertvector(v, bf16x2));
}
__device__ __forceinline__ float bflo(unsigned u) { return __builtin_bit_cast(float, u << 16); }
__device__ __forceinline__ float bfhi(unsigned u) { return __builtin_bit_cast(float, u & 0xffff0000u); }
__device__ __forceinline__ float rbf(float a) {
  return bflo(pack2(a, 0.f));
}
__device__ __forceinline__ float ln_mean(float m) {
  return m;
}
__device__ __forceinline__ float ln_rstd(float m2) {
  float v = m2 * (1.0f / D_);
  return 1.0f / sqrtf(v + 1e-5f);
}
__device__ __forceinline__ void glds16(const void* g, LAS void* l) {
  __builtin_amdgcn_global_load_lds((const GAS void*)g, l, 16, 0, 0);
}

#define SB() __builtin_amdgcn_sched_barrier(0)
#define STAMP(i)
#define STAMP_DECL
#define STAMP_OUT(k)
__device__ __forceinline__ void mfma_vvv(f32x4& c, v4u a, v4u b) {
  asm volatile("v_mfma_f32_16x16x32_bf16 %0, %1, %2, %0" : "+v"(c) : "v"(a), "v"(b));
}
__device__ __forceinline__ void mfma_avv(f32x4& c, v4u a, v4u b) {
  asm volatile("v_mfma_f32_16x16x32_bf16 %0, %1, %2, %0" : "+a"(c) : "v"(a), "v"(b));
}
template <bool FIRST, bool BA>
__device__ __forceinline__ void mfma_2(f32x4& c, v4u a, v4u b) {
  if (FIRST) {
    if (BA) asm volatile("v_mfma_f32_16x16x32_bf16 %0, %1, %2, 0" : "=&v"(c) : "v"(a), "a"(b));
    else asm volatile("v_mfma_f32_16x16x32_bf16 %0, %1, %2, 0" : "=&v"(c) : "v"(a), "v"(b));
  } else {
    if (BA) asm volatile("v_mfma_f32_16x16x32_bf16 %0, %1, %2, %0" : "+v"(c) : "v"(a), "a"(b));
    else asm volatile("v_mfma_f32_16x16x32_bf16 %0, %1, %2, %0" : "+v"(c) : "v"(a), "v"(b));
  }
}
__device__ __forceinline__ float relu(float x) {
  float r;
  asm("v_max_f32 %0, 0, %1" : "=v"(r) : "v"(x));
  return r;
}
__global__ __launch_bounds__(256) void k_prep(const u16* __restrict__ x, const u16* __restrict__ wqkv, const u16* __restrict__ wo,
                                              const u16* __restrict__ wg, const u16* __restrict__ wu, const u16* __restrict__ wd,
                                              unsigned char* __restrict__ ws) {
  __shared__ float red[4][16];
  __shared__ float red2[16];
  const int tid = threadIdx.x, lane = tid & 63, w = tid >> 6, r = lane & 15, q = lane >> 4;
  int u = blockIdx.x;
  if (u < 576) {
    const int run = u / 9, a = u % 9;
    const int f = 72 * run + 8 * a + 2 * (r >> 2) + (r & 1);
    const u16* src = ((r & 2) ? wu : wg) + f;
    float cs = 0.f;
    for (int s = w; s < KS; s += 4) {
      unsigned e[8];
#pragma unroll
      for (int j = 0; j < 8; ++j) e[j] = src[(size_t)(32 * s + 8 * q + j) * F_];
      v4u o;
#pragma unroll
      for (int j = 0; j < 4; ++j) { o[j] = e[2 * j] | (e[2 * j + 1] << 16); cs += bflo(e[2 * j]) + bflo(e[2 * j + 1]); }
      *(v4u*)(ws + OFF_WF + ((size_t)run * 243 + s * 9 + a) * 1024 + lane * 16) = o;
    }
    cs += __shfl_xor(cs, 16); cs += __shfl_xor(cs, 32);
    if (lane < 16) red[w][lane] = cs;
    __syncthreads();
    if (tid < 16) ((float*)(ws + OFF_CSG))[u * 16 + tid] = red[0][tid] + red[1][tid] + red[2][tid] + red[3][tid];
    return;
  }
  u -= 576;
  if (u < 576) {
    const int run = u / 9, a = u % 9, j16 = run >> 2, wv = run & 3;
    const int T = (wv & 1) ? (a < 8 ? 9 * wv + 1 + a : 9 * wv) : 9 * wv + a;
    const int n = 32 * (T >> 1) + 8 * (r >> 2) + 4 * (T & 1) + (r & 3);
    for (int s = w; s < 9; s += 4) {
      unsigned e[8];
#pragma unroll
      for (int j = 0; j < 8; ++j) {
        int ww, aa;
        if (s < 8) { ww = s >> 1; aa = 4 * (s & 1) + (j >> 1); } else { ww = j >> 1; aa = 8; }
        const int f = 288 * j16 + 72 * ww + 8 * aa + 2 * q + (j & 1);
        e[j] = wd[(size_t)f * D_ + n];
      }
      v4u o;
#pragma unroll
      for (int j = 0; j < 4; ++j) o[j] = e[2 * j] | (e[2 * j + 1] << 16);
      *(v4u*)(ws + OFF_WF + ((size_t)run * 243 + 162 + (a < 8 ? (a >> 1) * 18 + s * 2 + (a & 1) : 72 + s)) * 1024 + lane * 16) = o;
    }
    return;
  }
  u -= 576;
  if (u < 128) {
    const int h = u >> 2, a = u & 3, rho = 16 * a + r;
    int col = rho < 18 ? 576 + 18 * h + rho : rho < 36 ? 1152 + 18 * h + rho - 18 : 18 * h + rho - 36;
    const bool ok = rho < 54;
    if (!ok) col = 0;
    float cs = 0.f;
    for (int s = w; s < KS; s += 4) {
      unsigned e[8];
#pragma unroll
      for (int j = 0; j < 8; ++j) { unsigned t = wqkv[(size_t)(32 * s + 8 * q + j) * 1728 + col]; e[j] = ok ? t : 0u; }
      v4u o;
#pragma unroll
      for (int j = 0; j < 4; ++j) { o[j] = e[2 * j] | (e[2 * j + 1] << 16); cs += bflo(e[2 * j]) + bflo(e[2 * j + 1]); }
      *(v4u*)(ws + OFF_WQ + ((size_t)h * 72 + s * 4 + a) * 1024 + lane * 16) = o;
    }
    cs += __shfl_xor(cs, 16); cs += __shfl_xor(cs, 32);
    if (lane < 16) red[w][lane] = cs;
    __syncthreads();
    if (tid < 16) ((float*)(ws + OFF_CSQ))[u * 16 + tid] = red[0][tid] + red[1][tid] + red[2][tid] + red[3][tid];
    return;
  }
  u -= 128;
  if (u < 36) {
    const int T = u, n = 16 * T + r;
    for (int s = w; s < KS; s += 4) {
      const int g = 4 * s + q, hg = g / 9, i = g % 9;
      unsigned e[8];
#pragma unroll
      for (int j = 0; j < 8; ++j) {
        const int c = i < 8 ? (4 * hg + (i >> 1)) * 18 + (i & 1) * 8 + j : (4 * hg + (j >> 1)) * 18 + 16 + (j & 1);
        e[j] = wo[(size_t)c * D_ + n];
      }
      v4u o;
#pragma unroll
      for (int j = 0; j < 4; ++j) o[j] = e[2 * j] | (e[2 * j + 1] << 16);
      *(v4u*)(ws + OFF_WO + ((size_t)((T / 3) * 18 + s) * 3 + T % 3) * 1024 + lane * 16) = o;
    }
    return;
  }
  u -= 36;
  {
    v4u v[5];
    float sum = 0.f;
#pragma unroll
    for (int i = 0; i < 5; ++i) {
      const int s = w + 4 * i;
      if (s < KS) {
        v[i] = *(const v4u*)(x + (size_t)(16 * u + r) * D_ + 32 * s + 8 * q);
        *(v4u*)(ws + OFF_X0 + (size_t)u * TT_BYTES + s * 1024 + lane * 16) = v[i];
#pragma unroll
        for (int j = 0; j < 4; ++j) sum += bflo(v[i][j]) + bfhi(v[i][j]);
      }
    }
    sum += __shfl_xor(sum, 16); sum += __shfl_xor(sum, 32);
    if (lane < 16) red[w][lane] = sum;
    __syncthreads();
    const float mean = (red[0][r] + red[1][r] + red[2][r] + red[3][r]) * (1.0f / D_);
    float m2 = 0.f;
#pragma unroll
    for (int i = 0; i < 5; ++i) {
      const int s = w + 4 * i;
      if (s < KS) {
#pragma unroll
        for (int j = 0; j < 4; ++j) { float a = bflo(v[i][j]) - mean, b = bfhi(v[i][j]) - mean; m2 += a * a + b * b; }
      }
    }
    m2 += __shfl_xor(m2, 16); m2 += __shfl_xor(m2, 32);
    __syncthreads();
    if (lane < 16) red[w][lane] = m2;
    if (tid < 16) red2[tid] = mean;
    __syncthreads();
    if (tid < 16) {
      const float t = red[0][tid] + red[1][tid] + red[2][tid] + red[3][tid];
      f32x2 o = {ln_mean(red2[tid]), ln_rstd(t)};
      ((f32x2*)(ws + OFF_ST))[16 * u + tid] = o;
    }
  }
}

constexpr int QS = 88;
constexpr int K1_LDS = 90 * 1024 + 4 * 27 * QS * 4;
__global__ __launch_bounds__(256, 1) void k_qkv_attn(unsigned char* __restrict__ ws) {
  extern __shared__ __attribute__((aligned(16))) unsigned char smem_[];
  LAS unsigned char* L = (LAS unsigned char*)smem_;
  const int tid = threadIdx.x, lane = tid & 63, w = __builtin_amdgcn_readfirstlane(tid >> 6), tok = lane & 15, q = lane >> 4;
  const int b = blockIdx.x, hg = b & 7, mt = b >> 3;
  const int h = 4 * hg + w;
  const int tt0 = 4 * mt - 1;
  for (int i = w; i < 90; i += 4) {
    const int s = i / 5, t = i % 5;
    int tt = tt0 + t; tt = tt < 0 ? 0 : tt;
    glds16(ws + OFF_X0 + (size_t)tt * TT_BYTES + s * 1024 + lane * 16, L + i * 1024);
  }
  const GAS v4u* wp = (const GAS v4u*)(ws + OFF_WQ + (size_t)h * 72 * 1024) + lane;
  constexpr int WIN = 36;
  v4u ring[WIN];
#pragma unroll
  for (int i = 0; i < WIN; ++i) ring[i] = wp[i * 64];
  SB();
  float mean[5], rstd[5];
#pragma unroll
  for (int t = 0; t < 5; ++t) {
    int tt = tt0 + t; tt = tt < 0 ? 0 : tt;
    const f32x2 st = ((const GAS f32x2*)(ws + OFF_ST))[16 * tt + tok];
    mean[t] = st[0]; rstd[t] = st[1];
  }
  f32x4 acc[4][5];
#pragma unroll
  for (int a = 0; a < 4; ++a) {
    const f32x4 cs = *(const GAS f32x4*)(ws + OFF_CSQ + (size_t)(h * 64 + a * 16 + 4 * q) * 4);
#pragma unroll
    for (int t = 0; t < 5; ++t) { acc[a][t] = cs * (-mean[t]); asm volatile("" : "+v"(acc[a][t])); }
  }
  asm volatile("s_waitcnt vmcnt(0)" ::: "memory");
  __syncthreads();
#pragma unroll
  for (int s = 0; s < KS; ++s) {
    v4u B[5];
#pragma unroll
    for (int t = 0; t < 5; ++t) B[t] = *(LAS v4u*)(L + (s * 5 + t) * 1024 + lane * 16);
#pragma unroll
    for (int a = 0; a < 4; ++a) {
      const int i = s * 4 + a;
#pragma unroll
      for (int t = 0; t < 5; ++t)
        if (!(a == 3 && t == 0)) mfma_vvv(acc[a][t], ring[i % WIN], B[t]);
      if (i + WIN < 72) ring[i % WIN] = wp[(i + WIN) * 64];
    }
  }
  SB();
  asm volatile("s_nop 7" : "+v"(acc[2][0]), "+v"(acc[2][1]), "+v"(acc[2][2]), "+v"(acc[2][3]), "+v"(acc[2][4]), "+v"(acc[3][1]), "+v"(acc[3][2]), "+v"(acc[3][3]), "+v"(acc[3][4]));
  SB();
  LAS unsigned* E = (LAS unsigned*)(L + 90 * 1024) + w * 27 * QS;
#pragma unroll
  for (int a = 0; a < 4; ++a)
#pragma unroll
    for (int t = 0; t < 5; ++t) {
      if (a == 3 && t == 0) continue;
      const f32x4 v = acc[a][t] * rstd[t];
      const int p = 8 * a + 2 * q;
      if (p < 27) E[p * QS + 16 * t + tok] = pack2(v[0], v[1]);
      if (p + 1 < 27) E[(p + 1) * QS + 16 * t + tok] = pack2(v[2], v[3]);
    }
  __builtin_amdgcn_fence(__ATOMIC_RELEASE, "wavefront");
  __builtin_amdgcn_wave_barrier();
  __builtin_amdgcn_fence(__ATOMIC_ACQUIRE, "wavefront");
  const int slot = 16 + lane, pos = 64 * mt + lane;
  unsigned qv[9];
#pragma unroll
  for (int p = 0; p < 9; ++p) qv[p] = E[(18 + p) * QS + slot];
  float lg[8];
  const float sc = 0.23570226039551584f * 1.4426950408889634f;
#pragma unroll
  for (int i = 0; i < 8; ++i) {
    float d = 0.f;
#pragma unroll
    for (int p = 0; p < 9; ++p) {
      const unsigned kv = E[p * QS + slot - 2 * i];
      d = __builtin_amdgcn_fdot2_f32_bf16(__builtin_bit_cast(bf16x2, qv[p]), __builtin_bit_cast(bf16x2, kv), d, false);
    }
    lg[i] = (pos - 2 * i >= 0) ? d * sc : -1e30f;
  }
  float mx = lg[0];
#pragma unroll
  for (int i = 1; i < 8; ++i) mx = fmaxf(mx, lg[i]);
  float sum = 0.f;
#pragma unroll
  for (int i = 0; i < 8; ++i) { lg[i] = __builtin_amdgcn_exp2f(lg[i] - mx); sum += lg[i]; }
  const float inv = 1.0f / sum;
  float o[18];
#pragma unroll
  for (int d = 0; d < 18; ++d) o[d] = 0.f;
#pragma unroll
  for (int i = 0; i < 8; ++i) {
    const float pw = lg[i] * inv;
#pragma unroll
    for (int p = 0; p < 9; ++p) {
      const unsigned vv = E[(9 + p) * QS + slot - 2 * i];
      o[2 * p] += pw * bflo(vv);
      o[2 * p + 1] += pw * bfhi(vv);
    }
  }
  unsigned char* ob = ws + OFF_O + (size_t)(4 * mt + q) * TT_BYTES + tok * 16;
  v4u p0 = {pack2(o[0], o[1]), pack2(o[2], o[3]), pack2(o[4], o[5]), pack2(o[6], o[7])};
  v4u p1 = {pack2(o[8], o[9]), pack2(o[10], o[11]), pack2(o[12], o[13]), pack2(o[14], o[15])};
  *(v4u*)(ob + (9 * hg + 2 * w) * 256) = p0;
  *(v4u*)(ob + (9 * hg + 2 * w + 1) * 256) = p1;
  *(unsigned*)(ob + (9 * hg + 8) * 256 + 4 * w) = pack2(o[16], o[17]);
}

__global__ __launch_bounds__(192, 1) void k_wo(unsigned char* __restrict__ ws) {
  __shared__ float red[3][32];
  __shared__ float red2[3][32];
  const int tid = threadIdx.x, lane = tid & 63, w = __builtin_amdgcn_readfirstlane(tid >> 6), tok = lane & 15, q = lane >> 4;
  const int b = blockIdx.x, nb = b & 3, mt = b >> 2;
  const GAS v4u* wp = (const GAS v4u*)(ws + OFF_WO + (size_t)(3 * nb + w) * 54 * 1024) + lane;
  const GAS v4u* bp = (const GAS v4u*)(ws + OFF_O + (size_t)(2 * mt) * TT_BYTES) + lane;
  f32x4 acc[3][2];
  v4u A[54], B[36];
#pragma unroll
  for (int s = 0; s < KS; ++s) {
    B[2 * s] = bp[s * 64]; B[2 * s + 1] = bp[(KS + s) * 64];
#pragma unroll
    for (int a = 0; a < 3; ++a) A[3 * s + a] = wp[(3 * s + a) * 64];
  }
  v2u res[3][2];
#pragma unroll
  for (int a = 0; a < 3; ++a)
#pragma unroll
    for (int t = 0; t < 2; ++t) {
      const int T = 9 * nb + 3 * w + a;
      res[a][t] = *(const GAS v2u*)(ws + OFF_X0 + (size_t)(2 * mt + t) * TT_BYTES + T * 512 + ((q >> 1) * 16 + tok) * 16 + (q & 1) * 8);
    }
  __builtin_amdgcn_sched_barrier(0);
#pragma unroll
  for (int s = 0; s < KS; ++s)
#pragma unroll
    for (int a = 0; a < 3; ++a) {
      if (s == 0) { mfma_2<true, true>(acc[a][0], A[3 * s + a], B[2 * s]); mfma_2<true, true>(acc[a][1], A[3 * s + a], B[2 * s + 1]); }
      else { mfma_2<false, true>(acc[a][0], A[3 * s + a], B[2 * s]); mfma_2<false, true>(acc[a][1], A[3 * s + a], B[2 * s + 1]); }
    }
  asm volatile("s_nop 7" : "+v"(acc[0][0]), "+v"(acc[0][1]), "+v"(acc[1][0]), "+v"(acc[1][1]), "+v"(acc[2][0]), "+v"(acc[2][1]));
  float xv[2][12];
  float sum[2] = {0.f, 0.f};
#pragma unroll
  for (int a = 0; a < 3; ++a)
#pragma unroll
    for (int t = 0; t < 2; ++t) {
      const int T = 9 * nb + 3 * w + a;
      f32x4 y = acc[a][t];
      v2u o;
      o[0] = pack2(bflo(res[a][t][0]) + y[0], bfhi(res[a][t][0]) + y[1]);
      o[1] = pack2(bflo(res[a][t][1]) + y[2], bfhi(res[a][t][1]) + y[3]);
      *(v2u*)(ws + OFF_X1 + (size_t)(2 * mt + t) * TT_BYTES + T * 512 + ((q >> 1) * 16 + tok) * 16 + (q & 1) * 8) = o;
      xv[t][4 * a] = bflo(o[0]); xv[t][4 * a + 1] = bfhi(o[0]); xv[t][4 * a + 2] = bflo(o[1]); xv[t][4 * a + 3] = bfhi(o[1]);
      sum[t] += (xv[t][4 * a] + xv[t][4 * a + 1]) + (xv[t][4 * a + 2] + xv[t][4 * a + 3]);
    }
#pragma unroll
  for (int t = 0; t < 2; ++t) {
    sum[t] += __shfl_xor(sum[t], 16); sum[t] += __shfl_xor(sum[t], 32);
    if (lane < 16) red[w][16 * t + lane] = sum[t];
  }
  __syncthreads();
  float mean[2];
#pragma unroll
  for (int t = 0; t < 2; ++t) {
    mean[t] = (red[0][16 * t + tok] + red[1][16 * t + tok] + red[2][16 * t + tok]) * (1.0f / 144);
    float m2 = 0.f;
#pragma unroll
    for (int i = 0; i < 12; ++i) { const float d = xv[t][i] - mean[t]; m2 += d * d; }
    m2 += __shfl_xor(m2, 16); m2 += __shfl_xor(m2, 32);
    if (lane < 16) red2[w][16 * t + lane] = m2;
  }
  __syncthreads();
  if (tid < 32) {
    const int token = 32 * mt + tid;
    ((float*)(ws + OFF_PM))[token * 4 + nb] = (red[0][tid] + red[1][tid] + red[2][tid]) * (1.0f / 144);
    ((float*)(ws + OFF_PV))[token * 4 + nb] = red2[0][tid] + red2[1][tid] + red2[2][tid];
  }
}

constexpr int FFN_LDS = 152 * 1024;
template <int R0>
__device__ __forceinline__ void kstep(f32x4 (&acc)[9][8], v4u (&ring)[18], const v4u (&B)[8], const GAS v4u* wp) {
#pragma unroll
  for (int a = 0; a < 9; ++a) {
#pragma unroll
    for (int t = 0; t < 8; ++t) {
      if (a == 0) mfma_vvv(acc[0][t], ring[R0], B[t]);
      else mfma_avv(acc[a][t], ring[R0 + a], B[t]);
    }
    ring[R0 + a] = wp[(R0 + a) * 64];
    SB();
  }
}
__device__ __forceinline__ void ldB(v4u (&B)[8], LAS unsigned char* lp) {
#pragma unroll
  for (int t = 0; t < 8; ++t) B[t] = *(LAS v4u*)(lp + t * 1024);
  SB();
}
__global__ __launch_bounds__(256, 1) void k_ffn(unsigned char* __restrict__ ws) {
  extern __shared__ __attribute__((aligned(16))) unsigned char smem_[];
  LAS unsigned char* L = (LAS unsigned char*)smem_;
  const int tid = threadIdx.x, lane = tid & 63, w = __builtin_amdgcn_readfirstlane(tid >> 6), tok = lane & 15, q = lane >> 4;
  const int b = blockIdx.x, j16 = (b & 7) * 2 + ((b >> 3) & 1), m = b >> 4;
  STAMP_DECL;
  STAMP(0);
  for (int i = w; i < 144; i += 4) {
    const int s = i >> 3, t = i & 7;
    glds16(ws + OFF_X1 + (size_t)(8 * m + t) * TT_BYTES + s * 1024 + lane * 16, L + i * 1024);
  }
  const int run = 4 * j16 + w;
  const GAS v4u* wp = (const GAS v4u*)(ws + OFF_WF + (size_t)run * 243 * 1024) + lane;
  v4u ring[18];
#pragma unroll
  for (int i = 0; i < 18; ++i) ring[i] = wp[i * 64];
  f32x4 acc[9][8];
  {
    float mean[8];
#pragma unroll
    for (int t = 0; t < 8; ++t) {
      const f32x4 pm = ((const GAS f32x4*)(ws + OFF_PM))[128 * m + 16 * t + tok];
      mean[t] = -ln_mean(((pm[0] + pm[1]) + (pm[2] + pm[3])) * 0.25f);
    }
#pragma unroll
    for (int a = 0; a < 9; ++a) {
      const f32x4 cs = *(const GAS f32x4*)(ws + OFF_CSG + (size_t)((run * 9 + a) * 16 + 4 * q) * 4);
#pragma unroll
      for (int t = 0; t < 8; ++t) {
        acc[a][t] = cs * mean[t];
        if (a == 0) asm volatile("" : "+v"(acc[a][t])); else asm volatile("" : "+a"(acc[a][t]));
      }
      SB();
    }
  }
  STAMP(1);
  asm volatile("s_waitcnt vmcnt(0)" ::: "memory");
  __syncthreads();
  STAMP(2);
  LAS unsigned char* lp = L + lane * 16;
  {
    v4u B0[8], B1[8];
    ldB(B0, lp);
    wp += 18 * 64;
#pragma unroll 1
    for (int it = 0; it < 9; ++it) {
      ldB(B1, lp + 8192);
      kstep<0>(acc, ring, B0, wp);
      lp += 16 * 1024;
      ldB(B0, lp);
      kstep<9>(acc, ring, B1, wp);
      wp += 18 * 64;
    }
  }
  asm volatile("s_nop 7" : "+a"(acc[8][0]), "+a"(acc[8][1]), "+a"(acc[8][2]), "+a"(acc[8][3]), "+a"(acc[8][4]), "+a"(acc[8][5]), "+a"(acc[8][6]), "+a"(acc[8][7]));
  SB();
  STAMP(3);
  unsigned hreg[9][8];
#pragma unroll
  for (int a = 0; a < 9; ++a) {
#pragma unroll
    for (int t = 0; t < 8; ++t) {
      const f32x4 v = acc[a][t];
      hreg[a][t] = pack2(relu(v[0]) * v[2], relu(v[1]) * v[3]);
    }
    SB();
  }
  STAMP(4);
  __syncthreads();
  lp = L + lane * 16;
#pragma unroll
  for (int t = 0; t < 8; ++t) {
    *(LAS v4u*)(lp + ((2 * w) * 8 + t) * 1024) = v4u{hreg[0][t], hreg[1][t], hreg[2][t], hreg[3][t]};
    *(LAS v4u*)(lp + ((2 * w + 1) * 8 + t) * 1024) = v4u{hreg[4][t], hreg[5][t], hreg[6][t], hreg[7][t]};
    *(LAS unsigned*)(lp + (64 + t) * 1024 + 4 * w) = hreg[8][t];
  }
  __syncthreads();
  v4u Hf[9][8];
#pragma unroll
  for (int s = 0; s < 9; ++s)
#pragma unroll
    for (int t = 0; t < 8; ++t) {
      Hf[s][t] = *(LAS v4u*)(lp + (s * 8 + t) * 1024);
    }
  SB();
  STAMP(5);
  unsigned char* pb = ws + OFF_P + (size_t)j16 * XF_BYTES + (size_t)(8 * m) * TT_BYTES + lane * 16;
  const int s0 = (9 * w + (w & 1)) >> 1;
  const int T8 = (w & 1) ? 9 * w : 9 * w + 8;
  unsigned char* pp = pb + s0 * 1024;
#pragma unroll 1
  for (int p = 0; p < 4; ++p) {
    f32x4 c0[8], c1[8];
#pragma unroll
    for (int s = 0; s < 8; ++s) {
#pragma unroll
      for (int t = 0; t < 8; ++t) { if (s == 0) mfma_2<true, false>(c0[t], ring[2 * s], Hf[s][t]); else mfma_2<false, true>(c0[t], ring[2 * s], Hf[s][t]); }
      ring[2 * s] = wp[(2 * s) * 64];
      SB();
#pragma unroll
      for (int t = 0; t < 8; ++t) { if (s == 0) mfma_2<true, false>(c1[t], ring[2 * s + 1], Hf[s][t]); else mfma_2<false, true>(c1[t], ring[2 * s + 1], Hf[s][t]); }
      ring[2 * s + 1] = wp[(2 * s + 1) * 64];
      SB();
    }
#pragma unroll
    for (int t = 0; t < 12; ++t) {
      if (t < 8) {
        mfma_2<false, true>(c0[t], ring[16], Hf[8][t]);
        mfma_2<false, true>(c1[t], ring[17], Hf[8][t]);
      }
      if (t == 8) {
        ring[16] = wp[16 * 64];
        ring[17] = wp[17 * 64];
        asm volatile("s_nop 7");
      }
      SB();
      if (t >= 4) {
        const f32x4 u0 = c0[t - 4], u1 = c1[t - 4];
        *(v4u*)(pp + (size_t)(t - 4) * TT_BYTES) = v4u{pack2(u0[0], u0[1]), pack2(u0[2], u0[3]), pack2(u1[0], u1[1]), pack2(u1[2], u1[3])};
      }
      SB();
    }
    wp += 18 * 64;
    pp += 1024;
  }
  {
    f32x4 c0[8];
#pragma unroll
    for (int s = 0; s < 9; ++s) {
#pragma unroll
      for (int t = 0; t < 8; ++t) { if (s == 0) mfma_2<true, false>(c0[t], ring[s], Hf[s][t]); else mfma_2<false, true>(c0[t], ring[s], Hf[s][t]); }
      SB();
    }
    asm volatile("s_nop 7");
    SB();
#pragma unroll
    for (int t = 0; t < 8; ++t) {
      const f32x4 u8 = c0[t];
      *(v2u*)(pb + (size_t)t * TT_BYTES + (T8 >> 1) * 1024 + (T8 & 1) * 8) = v2u{pack2(u8[0], u8[1]), pack2(u8[2], u8[3])};
    }
  }
  STAMP(6);
  STAMP(7);
  STAMP_OUT(0);
}

template <bool LAST>
__global__ __launch_bounds__(576, 1) void k_reduce(unsigned char* __restrict__ ws, u16* __restrict__ out) {
  __shared__ float red[9][8];
  __shared__ float red2[9][8];
  const int tid = threadIdx.x, lane = tid & 63, w = tid >> 6, tk = tid & 7, g = tid >> 3;
  const int b = blockIdx.x, tt = b >> 1, token = 8 * b + tk;
  const size_t off = (size_t)tt * TT_BYTES + g * 256 + (8 * (b & 1) + tk) * 16;
  v4u pv[16];
#pragma unroll
  for (int j = 0; j < 16; ++j) pv[j] = *(const GAS v4u*)(ws + OFF_P + (size_t)j * XF_BYTES + off);
  const v4u xr = *(const GAS v4u*)(ws + OFF_X1 + off);
  const f32x4 pm = ((const GAS f32x4*)(ws + OFF_PM))[token];
  const f32x4 p2 = ((const GAS f32x4*)(ws + OFF_PV))[token];
  const float mu = ((pm[0] + pm[1]) + (pm[2] + pm[3])) * 0.25f;
  float m2 = (p2[0] + p2[1]) + (p2[2] + p2[3]);
#pragma unroll
  for (int i = 0; i < 4; ++i) m2 += 144.f * (pm[i] - mu) * (pm[i] - mu);
  const float r = ln_rstd(m2), r2 = r * r;
  float acc[8];
#pragma unroll
  for (int i = 0; i < 8; ++i) acc[i] = 0.f;
#pragma unroll
  for (int j = 0; j < 16; ++j)
#pragma unroll
    for (int i = 0; i < 4; ++i) { acc[2 * i] += bflo(pv[j][i]); acc[2 * i + 1] += bfhi(pv[j][i]); }
  v4u o;
  float sum = 0.f;
#pragma unroll
  for (int i = 0; i < 4; ++i) {
    float y0 = acc[2 * i] * r2, y1 = acc[2 * i + 1] * r2;
    o[i] = pack2(bflo(xr[i]) + y0, bfhi(xr[i]) + y1);
    sum += bflo(o[i]) + bfhi(o[i]);
  }
  if (LAST) {
    *(v4u*)(out + (size_t)token * D_ + g * 8) = o;
    return;
  }
  *(v4u*)(ws + OFF_X0 + off) = o;
  sum += __shfl_xor(sum, 8); sum += __shfl_xor(sum, 16); sum += __shfl_xor(sum, 32);
  if (lane < 8) red[w][lane] = sum;
  __syncthreads();
  float mean = 0.f;
#pragma unroll
  for (int i = 0; i < 9; ++i) mean += red[i][tk];
  mean *= (1.0f / D_);
  float q2 = 0.f;
#pragma unroll
  for (int i = 0; i < 4; ++i) { const float a = bflo(o[i]) - mean, c = bfhi(o[i]) - mean; q2 += a * a + c * c; }
  q2 += __shfl_xor(q2, 8); q2 += __shfl_xor(q2, 16); q2 += __shfl_xor(q2, 32);
  if (lane < 8) red2[w][lane] = q2;
  __syncthreads();
  if (tid < 8) {
    float t = 0.f;
#pragma unroll
    for (int i = 0; i < 9; ++i) t += red2[i][tid];
    f32x2 st = {ln_mean(mean), ln_rstd(t)};
    ((f32x2*)(ws + OFF_ST))[token] = st;
  }
}

extern "C" { int g_stop = 1000; size_t g_offs[16] = {OFF_WQ, OFF_WO, OFF_WF, OFF_CSQ, OFF_CSG, OFF_ST, OFF_PM, OFF_PV, OFF_X0, OFF_X1, OFF_O, OFF_P, WS_TOTAL}; }
extern "C" void kernel_launch(void* const* d_in, const int* in_sizes, int n_in, void* d_out, int out_size, void* d_ws,
                              size_t ws_size, hipStream_t stream) {
  const u16* x = (const u16*)d_in[0];
  const u16* wqkv = (const u16*)d_in[1];
  const u16* wo = (const u16*)d_in[2];
  const u16* wg = (const u16*)d_in[4];
  const u16* wu = (const u16*)d_in[5];
  const u16* wd = (const u16*)d_in[6];
  unsigned char* ws = (unsigned char*)d_ws;
  (void)hipFuncSetAttribute((const void*)k_qkv_attn, hipFuncAttributeMaxDynamicSharedMemorySize, K1_LDS);
  (void)hipFuncSetAttribute((const void*)k_ffn, hipFuncAttributeMaxDynamicSharedMemorySize, FFN_LDS);
  int nk = 0;
#define STOPCHK() if (++nk >= g_stop) return
  k_prep<<<576 + 576 + 128 + 36 + 128, 256, 0, stream>>>(x, wqkv, wo, wg, wu, wd, ws);
  STOPCHK();
  for (int l = 0; l < 2; ++l) {
    k_qkv_attn<<<256, 256, K1_LDS, stream>>>(ws);
    STOPCHK();
    k_wo<<<256, 192, 0, stream>>>(ws);
    STOPCHK();
    k_ffn<<<256, 256, FFN_LDS, stream>>>(ws);
    STOPCHK();
    if (l == 0) k_reduce<false><<<256, 576, 0, stream>>>(ws, (u16*)d_out);
    else k_reduce<true><<<256, 576, 0, stream>>>(ws, (u16*)d_out);
    STOPCHK();
  }
}
```

```cpp
#include <hip/hip_runtime.h>
#include <hip/hip_bf16.h>
#include <stdint.h>
#include <type_traits>

#define GAS __attribute__((address_space(1)))
#define LAS __attribute__((address_space(3)))
typedef unsigned v4u __attribute__((ext_vector_type(4)));
typedef unsigned v3u __attribute__((ext_vector_type(3)));
typedef unsigned v2u __attribute__((ext_vector_type(2)));
typedef float f32x4 __attribute__((ext_vector_type(4)));
typedef float f32x2 __attribute__((ext_vector_type(2)));
typedef __bf16 bf16x2 __attribute__((ext_vector_type(2)));
typedef unsigned short u16;
typedef __amdgpu_buffer_rsrc_t rsrc_t;

constexpr int S_ = 2048, D_ = 576, F_ = 4608;
constexpr int KS = 18;
constexpr int XF_BYTES = S_ * D_ * 2;
constexpr int TT_BYTES = KS * 1024;
constexpr int RUN_FR = 486;

constexpr int OFF_CNT = 0;
constexpr int OFF_TMO = 4 * 32 * 256;
constexpr int CNT_BYTES = OFF_TMO + 256;
constexpr int OFF_WQ = 65536;
constexpr int OFF_WO = OFF_WQ + 32 * 72 * 1024;
constexpr int OFF_WF = OFF_WO + 36 * 18 * 1024;
constexpr int OFF_CSG = OFF_WF + 32 * RUN_FR * 1024;
constexpr int OFF_SC = OFF_CSG + 32 * 18 * 16 * 4;
constexpr int SC_BYTES = 2 * 2048 * 48;
constexpr int OFF_X = OFF_SC + 2 * SC_BYTES;
constexpr int OFF_XN = OFF_X + 2 * XF_BYTES;
constexpr int OFF_X1 = OFF_XN + 2 * XF_BYTES;
constexpr int OFF_O = OFF_X1 + 2 * XF_BYTES;
constexpr int OFF_P = OFF_O + 2 * XF_BYTES;
constexpr int WS_TOTAL = OFF_P + 16 * XF_BYTES;
static_assert(OFF_X % 256 == 0 && OFF_SC % 256 == 0, "alignment");
constexpr int OOB = 0x70000000;

#define MIMIC 0
constexpr int SC1 = 16;

#define SB() __builtin_amdgcn_sched_barrier(0)
#define BAR() asm volatile("s_barrier" ::: "memory")
#define DRAIN() do { __builtin_amdgcn_s_waitcnt(0x0F70); asm volatile("s_waitcnt vmcnt(0)" ::: "memory"); } while (0)
#define STAMP(i)

__device__ __forceinline__ unsigned pack2(float a, float b) {
  f32x2 v = {a, b};
  return __builtin_bit_cast(unsigned, __builtin_convertvector(v, bf16x2));
}
__device__ __forceinline__ float asf(unsigned u) { return __builtin_bit_cast(float, u); }
__device__ __forceinline__ float bflo(unsigned u) { return __builtin_bit_cast(float, u << 16); }
__device__ __forceinline__ float bfhi(unsigned u) { return __builtin_bit_cast(float, u & 0xffff0000u); }
__device__ __forceinline__ float rbf(float a) { return bflo(pack2(a, 0.f)); }
__device__ __forceinline__ float ln_mean(float m) { return MIMIC ? rbf(m) : m; }
__device__ __forceinline__ float ln_rstd(float m2) {
  const float v = m2 * (1.0f / D_);
  return MIMIC ? 1.0f / rbf(sqrtf(rbf(rbf(v) + 1e-5f))) : __builtin_amdgcn_rsqf(v + 1e-5f);
}
__device__ __forceinline__ float sum_q(float x) {
  unsigned u = __builtin_bit_cast(unsigned, x);
  auto r = __builtin_amdgcn_permlane16_swap(u, u, false, false);
  x = asf(r[0]) + asf(r[1]);
  u = __builtin_bit_cast(unsigned, x);
  auto s = __builtin_amdgcn_permlane32_swap(u, u, false, false);
  return asf(s[0]) + asf(s[1]);
}
__device__ __forceinline__ float add_x8(float x) {
  return x + __builtin_bit_cast(float, __builtin_amdgcn_update_dpp(0, __builtin_bit_cast(int, x), 0x128, 0xf, 0xf, false));
}
__device__ __forceinline__ float dot2(unsigned a, unsigned b, float c) {
  return __builtin_amdgcn_fdot2_f32_bf16(__builtin_bit_cast(bf16x2, a), __builtin_bit_cast(bf16x2, b), c, false);
}
template <int I, int N, class Fn> __device__ __forceinline__ void sfor(Fn&& fn) {
  if constexpr (I < N) { fn(std::integral_constant<int, I>{}); sfor<I + 1, N>(fn); }
}

typedef short v4s __attribute__((ext_vector_type(4)));
constexpr int PREP_LDS = 72 * 1024;
constexpr int PREP_UNITS = 72 + 72 + 72 + 9 + 48 + 128;
__device__ __forceinline__ v4u tr_frag(LAS unsigned char* p0, LAS unsigned char* p1) {
  const v2u lo = __builtin_bit_cast(v2u, __builtin_amdgcn_ds_read_tr16_b64_v4i16((LAS v4s*)p0));
  const v2u hi = __builtin_bit_cast(v2u, __builtin_amdgcn_ds_read_tr16_b64_v4i16((LAS v4s*)p1));
  return v4u{lo[0], lo[1], hi[0], hi[1]};
}
__global__ __launch_bounds__(256, 2) void k_prep(const u16* __restrict__ x, const u16* __restrict__ wqkv, const u16* __restrict__ wo,
                                                 const u16* __restrict__ wg, const u16* __restrict__ wu, const u16* __restrict__ wd,
                                                 unsigned char* __restrict__ ws) {
  extern __shared__ __attribute__((aligned(16))) unsigned char smem_[];
  LAS unsigned char* L = (LAS unsigned char*)smem_;
  const unsigned Lb = (unsigned)(uintptr_t)L;
  const int tid = threadIdx.x, lane = tid & 63, w = __builtin_amdgcn_readfirstlane(tid >> 6), r = lane & 15, q = lane >> 4;
  const int u = blockIdx.x;
#define PST(i)
#define PEND()
  PST(0);
  if (u >= 273) {
    const int tt = u - 273;
    if (tt == 0 && tid <= 128) *(unsigned*)(ws + OFF_CNT + tid * 256) = 0u;
    LAS float* red = (LAS float*)L;
    v4u v[5];
    float sum = 0.f;
#pragma unroll
    for (int i = 0; i < 5; ++i) {
      const int s = w + 4 * i;
      v[i] = v4u{0u, 0u, 0u, 0u};
      if (s < KS) v[i] = *(const v4u*)(x + (size_t)(16 * tt + r) * D_ + 32 * s + 8 * q);
    }
#pragma unroll
    for (int i = 0; i < 5; ++i) {
      const int s = w + 4 * i;
      if (s < KS) *(v4u*)(ws + OFF_X + (size_t)tt * TT_BYTES + s * 1024 + lane * 16) = v[i];
#pragma unroll
      for (int e = 0; e < 4; ++e) sum += bflo(v[i][e]) + bfhi(v[i][e]);
    }
    sum = sum_q(sum);
    if (lane < 16) red[w * 16 + lane] = sum;
    __syncthreads();
    const float mean = (red[r] + red[16 + r] + red[32 + r] + red[48 + r]) * (1.0f / D_);
    float m2 = 0.f;
#pragma unroll
    for (int i = 0; i < 5; ++i)
      if (w + 4 * i < KS) {
#pragma unroll
        for (int e = 0; e < 4; ++e) { const float a = bflo(v[i][e]) - mean, b = bfhi(v[i][e]) - mean; m2 += a * a + b * b; }
      }
    m2 = sum_q(m2);
    if (lane < 16) red[64 + w * 16 + lane] = m2;
    __syncthreads();
    const float rstd = ln_rstd(red[64 + r] + red[80 + r] + red[96 + r] + red[112 + r]);
#pragma unroll
    for (int i = 0; i < 5; ++i) {
      const int s = w + 4 * i;
      v4u o;
#pragma unroll
      for (int e = 0; e < 4; ++e) o[e] = pack2((bflo(v[i][e]) - mean) * rstd, (bfhi(v[i][e]) - mean) * rstd);
      if (s < KS) *(v4u*)(ws + OFF_XN + (size_t)tt * TT_BYTES + s * 1024 + lane * 16) = o;
    }
    PEND();
    return;
  }
  if (u >= 225) {
    const int hg = (u - 225) & 7, kc = (u - 225) >> 3;
    const rsrc_t rs = __builtin_amdgcn_make_buffer_rsrc((void*)wqkv, 0, D_ * 1728 * 2, 0x00020000);
#pragma unroll
    for (int n = 0; n < 11; ++n) {
      const int i = 4 * n + w, pc = 64 * i + lane, row = pc / 27, c = pc % 27, part = c / 9;
      const int voff = row < 96 ? (96 * kc + row) * 3456 + ((part + 1) % 3) * 1152 + hg * 144 + (c % 9) * 16 : OOB;
      asm volatile("s_mov_b32 m0, %0\n\ts_nop 0\n\tbuffer_load_dwordx4 %1, %2, 0 offen lds" :: "s"(Lb + i * 1024), "v"(voff), "s"(rs) : "memory");
    }
    PST(1);
    DRAIN();
    BAR();
    PST(2);
    const int p = lane & 3, hb = 36 * w - 4 * (w & 1);
    LAS unsigned char* tp = L + (8 * q + (r >> 2)) * 432;
#pragma unroll
    for (int a = 0; a < 4; ++a) {
      const int cb = a == 2 ? (p < 3 ? p : 0) * 144 + hb + ((w & 1) ? 0 : 32) : (a == 3 ? 2 : a) * 144 + hb + 8 * (w & 1) + 8 * p;
#pragma unroll
      for (int s = 0; s < 3; ++s)
        *(v4u*)(ws + OFF_WQ + ((4 * hg + w) * 72 + 4 * (3 * kc + s) + a) * 1024 + lane * 16) = tr_frag(tp + cb + s * 32 * 432, tp + cb + (s * 32 + 4) * 432);
    }
    PEND();
    return;
  }
  int b0, st, b1, st1, csoff = -1, cbyte = 32 * w + 8 * (lane & 3);
  {
    const int type = u < 72 ? 0 : u < 144 ? 1 : u < 216 ? 2 : 3;
    const int cb = type < 2 ? u - 72 * type : type == 2 ? (u - 144) % 9 : u - 216;
    const int j8 = type == 2 ? (u - 144) / 9 : 0;
    const u16* src = type == 0 ? wg : type == 1 ? wu : type == 2 ? wd : wo;
    const int pitch = type < 2 ? F_ * 2 : D_ * 2;
    const rsrc_t rs = __builtin_amdgcn_make_buffer_rsrc((void*)src, 0, type == 3 ? D_ * D_ * 2 : D_ * F_ * 2, 0x00020000);
    const int jj = lane >> 3;
#pragma unroll
    for (int n = 0; n < 18; ++n) {
      const int i = (n / 6) * 24 + 6 * w + n % 6;
      int R;
      if (type < 2) R = 8 * i + jj;
      else if (type == 2) {
        const int s = i >> 2, qq = i & 3;
        R = 576 * j8 + (s < 16 ? 144 * (s >> 2) + 16 * (2 * (s & 3) + (jj >> 2)) : 144 * (2 * (s - 16) + (jj >> 2)) + 128) + 4 * qq + (jj & 3);
      } else {
        const int hg = i / 9, ii = i % 9;
        R = ii < 8 ? (4 * hg + (ii >> 1)) * 18 + (ii & 2) + (ii & 1) * 8 + jj : (4 * hg + (jj >> 1)) * 18 + ((jj & 2) ? 0 : 16) + (jj & 1);
      }
      const int voff = R * pitch + cb * 128 + (((lane & 7) ^ (2 * ((jj >> 1) & 1) + 4 * (i & 1))) << 4);
      asm volatile("s_mov_b32 m0, %0\n\ts_nop 0\n\tbuffer_load_dwordx4 %1, %2, 0 offen lds" :: "s"(Lb + i * 1024), "v"(voff), "s"(rs) : "memory");
    }
    const int T = 4 * cb + w;
    if (type < 2) {
      const int run = T / 9, i = T % 9;
      b0 = OFF_WF + (run * RUN_FR + 2 * i + type) * 1024; st = 18 * 1024;
      b1 = OFF_WF + (run * RUN_FR + 288 + 4 * i + type) * 1024; st1 = 2048;
      csoff = OFF_CSG + (run * 18 + 2 * i + type) * 64;
    } else if (type == 2) {
      const int wv = T / 9, a = (wv & 1) ? (T == 9 * wv ? 8 : T - 9 * wv - 1) : T - 9 * wv;
      b0 = OFF_WF + ((4 * j8 + wv) * RUN_FR + 324 + (a < 8 ? (a >> 1) * 36 + (a & 1) : 144)) * 1024; st = a < 8 ? 2048 : 1024;
      b1 = b0 + 16 * st; st1 = st;
      cbyte = 64 * (w >> 1) + 16 * (lane & 3) + 8 * (w & 1);
    } else {
      b0 = OFF_WO + ((T / 3) * 54 + T % 3) * 1024; st = 3072; b1 = b0 + 16 * st; st1 = st;
    }
  }
  PST(1);
  const int rr = r >> 2;
  LAS unsigned char* tp = L + (8 * q + rr) * 128 + ((((cbyte >> 4) ^ (2 * (rr >> 1) + 4 * (q & 1))) << 4) + (cbyte & 8));
  float cs = 0.f;
#pragma unroll
  for (int c = 0; c < 3; ++c) {
    asm volatile("s_waitcnt vmcnt(12)\n\ts_barrier" ::: "memory");
    PST(2 + c);
#pragma unroll
    for (int k = 0; k < 6; ++k) {
      const int s = 6 * c + k;
      const v4u o = tr_frag(tp + s * 4096, tp + s * 4096 + 512);
      *(v4u*)(ws + (s < 16 ? b0 + s * st : b1 + (s - 16) * st1) + lane * 16) = o;
#pragma unroll
      for (int e = 0; e < 4; ++e) cs = dot2(o[e], 0x3f803f80u, cs);
    }
  }
  cs = sum_q(cs);
  if (csoff >= 0 && lane < 16) *(float*)(ws + csoff + lane * 4) = cs;
  PEND();
}

template <int AUX> __device__ __forceinline__ v4u bload(rsrc_t rs, int voff, int soff) { return __builtin_amdgcn_raw_buffer_load_b128(rs, voff, soff, AUX); }
__device__ __forceinline__ void bstore(v4u v, rsrc_t rs, int voff, int soff) {
  __builtin_amdgcn_raw_buffer_store_b128(v, rs, voff, soff, SC1);
  asm volatile("s_nop 1" :: "v"(v) : "memory");
}
#define AQ(n) "a[%c" #n "*4:%c" #n "*4+3]"
template <int Q, int F> __device__ __forceinline__ void wload(rsrc_t rs, int lane16, int base) {
  asm volatile("buffer_load_dwordx4 " AQ(0) ", %1, %2, %3 offen offset:%4" :: "i"(Q), "v"(lane16), "s"(rs), "s"(base + (F >> 2) * 4096), "i"((F & 3) * 1024) : "memory");
}
template <int Q, int OFF> __device__ __forceinline__ void lload(LAS unsigned char* p) {
  asm volatile("ds_read_b128 " AQ(0) ", %1 offset:%2" :: "i"(Q), "v"(p), "i"(OFF) : "memory");
}
template <int N> __device__ __forceinline__ void wait_vm() { asm volatile("s_waitcnt vmcnt(%0)" :: "i"(N) : "memory"); }
template <int N> __device__ __forceinline__ void wait_lgkm() { asm volatile("s_waitcnt lgkmcnt(%0)" :: "i"(N) : "memory"); }
#define DMA1(o, c) "buffer_load_dwordx4 %1, %2, %3 offen offset:" #o c " lds\n\t"
template <int N, bool DEV = true> __device__ __forceinline__ void dma(rsrc_t rs, unsigned m0v, int voff, unsigned soff) {
  if constexpr (N == 2) asm volatile("s_mov_b32 m0, %0\n\ts_nop 0\n\t" DMA1(0, " sc1") DMA1(1024, " sc1") :: "s"(m0v), "v"(voff), "s"(rs), "s"(soff) : "memory");
  if constexpr (N == 3 && DEV) asm volatile("s_mov_b32 m0, %0\n\ts_nop 0\n\t" DMA1(0, " sc1") DMA1(1024, " sc1") DMA1(2048, " sc1") :: "s"(m0v), "v"(voff), "s"(rs), "s"(soff) : "memory");
  if constexpr (N == 3 && !DEV) asm volatile("s_mov_b32 m0, %0\n\ts_nop 0\n\t" DMA1(0, "") DMA1(1024, "") DMA1(2048, "") :: "s"(m0v), "v"(voff), "s"(rs), "s"(soff) : "memory");
}
__device__ __forceinline__ void dma2_if_lt6(int it, rsrc_t rs, unsigned m0v, int voff, unsigned soff) {
  asm volatile("s_cmp_ge_u32 %4, 6\n\ts_cbranch_scc1 .Lskip%=\n\ts_mov_b32 m0, %0\n\ts_nop 0\n\t" DMA1(0, " sc1") DMA1(1024, " sc1") ".Lskip%=:"
               :: "s"(m0v), "v"(voff), "s"(rs), "s"(soff), "s"(it) : "memory", "scc");
}
#define MFS "v_mfma_f32_16x16x32_bf16 "
template <int QA, bool FIRST = false> __device__ __forceinline__ void mf_av(f32x4& c, v4u b) {
  if constexpr (FIRST) asm volatile(MFS "%0, " AQ(1) ", %2, 0" : "=&v"(c) : "i"(QA), "v"(b));
  else asm volatile(MFS "%0, " AQ(1) ", %2, %0" : "+v"(c) : "i"(QA), "v"(b));
}
template <int QA, int QB> __device__ __forceinline__ void mf_aa(f32x4& c) { asm volatile(MFS "%0, " AQ(1) ", " AQ(2) ", %0" : "+v"(c) : "i"(QA), "i"(QB)); }
template <int QC, int QA, int QB> __device__ __forceinline__ void mf_aaa() { asm volatile(MFS AQ(0) ", " AQ(1) ", " AQ(2) ", " AQ(0) :: "i"(QC), "i"(QA), "i"(QB)); }
template <int QB, bool FIRST = false> __device__ __forceinline__ void mf_va(f32x4& c, v4u a) { asm volatile(MFS "%0, %2, " AQ(1) ", %0" : "+v"(c) : "i"(QB), "v"(a)); }
template <bool FIRST = false> __device__ __forceinline__ void mf_vv(f32x4& c, v4u a, v4u b) {
  if constexpr (FIRST) asm volatile(MFS "%0, %1, %2, 0" : "=&v"(c) : "v"(a), "v"(b));
  else asm volatile(MFS "%0, %1, %2, %0" : "+v"(c) : "v"(a), "v"(b));
}
template <int Q> __device__ __forceinline__ void quad_write(f32x4 v) {
  asm volatile("v_accvgpr_write_b32 a[%c0*4], %1\n\tv_accvgpr_write_b32 a[%c0*4+1], %2\n\tv_accvgpr_write_b32 a[%c0*4+2], %3\n\tv_accvgpr_write_b32 a[%c0*4+3], %4"
               :: "i"(Q), "v"(v[0]), "v"(v[1]), "v"(v[2]), "v"(v[3]));
}
constexpr bool acc_in_a(int a) { return a >= 2 && a < 6; }
constexpr int accq(int a, int t) { return 36 + (a - 2) * 4 + t; }
constexpr int BQ = 52;
__device__ __forceinline__ unsigned hpair(float g0, float g1, float u0, float u1) {
  unsigned h; float t;
  asm volatile("v_max_f32 %0, 0, %2\n\tv_max_f32 %1, 0, %3\n\tv_mul_f32 %0, %0, %4\n\tv_mul_f32 %1, %1, %5\n\tv_cvt_pk_bf16_f32 %0, %0, %1"
               : "=&v"(h), "=&v"(t) : "v"(g0), "v"(g1), "v"(u0), "v"(u1));
  return h;
}
__device__ __forceinline__ void hp1(float& x, float& y, float g0, float g1) { asm volatile("v_max_f32 %0, 0, %2\n\tv_max_f32 %1, 0, %3" : "=&v"(x), "=&v"(y) : "v"(g0), "v"(g1)); }
__device__ __forceinline__ unsigned hp2(float x, float y, float u0, float u1) {
  asm volatile("v_mul_f32 %0, %0, %2\n\tv_mul_f32 %1, %1, %3\n\tv_cvt_pk_bf16_f32 %0, %0, %1" : "+v"(x), "+v"(y) : "v"(u0), "v"(u1));
  return __builtin_bit_cast(unsigned, x);
}
template <int RG> __device__ __forceinline__ void hp1_a(float& x, float& y) {
  asm volatile("v_accvgpr_read_b32 %0, a[%c2]\n\tv_accvgpr_read_b32 %1, a[%c2+1]\n\tv_max_f32 %0, 0, %0\n\tv_max_f32 %1, 0, %1" : "=&v"(x), "=&v"(y) : "i"(RG));
}
template <int RU> __device__ __forceinline__ unsigned hp2_a(float x, float y) {
  float t;
  asm volatile("v_accvgpr_read_b32 %2, a[%c3]\n\tv_mul_f32 %0, %0, %2\n\tv_accvgpr_read_b32 %2, a[%c3+1]\n\tv_mul_f32 %1, %1, %2\n\tv_cvt_pk_bf16_f32 %0, %0, %1"
               : "+v"(x), "+v"(y), "=&v"(t) : "i"(RU));
  return __builtin_bit_cast(unsigned, x);
}
typedef GAS unsigned gu32;
#define RLX_AGENT __ATOMIC_RELAXED, __HIP_MEMORY_SCOPE_AGENT
__device__ __forceinline__ gu32* cnt_ptr(unsigned char* ws, int phase, int g) { return (gu32*)(ws + OFF_CNT + (phase * 32 + g) * 256); }
__device__ __forceinline__ void arrive(unsigned char* ws, int phase, int g, int lane) {
  if (lane == 0) (void)__hip_atomic_fetch_add(cnt_ptr(ws, phase, g), 1u, RLX_AGENT);
}
__device__ __forceinline__ void wait_cnt(unsigned char* ws, int phase, int g0, int g1, unsigned want, int lane) {
  gu32* p = lane == 2 ? (gu32*)(ws + OFF_TMO) : cnt_ptr(ws, phase, (lane & 1) ? g1 : g0);
  if (lane < 3) {
#pragma nounroll
    for (unsigned spins = 0;; ++spins) {
      const unsigned v = __hip_atomic_load(p, RLX_AGENT);
      const unsigned long long ok = __builtin_amdgcn_ballot_w64(v >= (lane == 2 ? 1u : want));
      if ((ok & 3ull) == 3ull || (ok & 4ull)) break;
      if (spins > (1u << 18)) { if (lane == 2) __hip_atomic_store(p, 1u, RLX_AGENT); break; }
    }
  }
  __builtin_amdgcn_fence(__ATOMIC_ACQUIRE, "wavefront");
}

constexpr int MAIN_LDS = 152 * 1024;
constexpr int EROW = 144;
constexpr int E_OFF = 90 * 1024;
constexpr int XS = 20 * 1024;
constexpr int TW_OFF = 4 * XS;

template <bool LAST>
__device__ __forceinline__ void phase_d(LAS unsigned char* L, rsrc_t rs, u16* out, int l, int g, int j, int w, int lane) {
  const int tk = lane & 7, gl = lane >> 3;
  const int dv = gl * 256 + tk * 16, db = (4 * g + (j >> 1)) * TT_BYTES + (j & 1) * 128 + w * 6144;
  v4u xr[3];
  f32x4 sm[3], sv[3];
#pragma unroll
  for (int i = 0; i < 3; ++i) {
    xr[i] = bload<SC1>(rs, dv, OFF_X1 + l * XF_BYTES + db + i * 2048);
    sm[i] = __builtin_bit_cast(f32x4, bload<SC1>(rs, tk * 48 + i * 16, OFF_SC + l * SC_BYTES + (64 * g + 8 * j) * 48));
    sv[i] = __builtin_bit_cast(f32x4, bload<SC1>(rs, tk * 48 + i * 16, OFF_SC + l * SC_BYTES + 2048 * 48 + (64 * g + 8 * j) * 48));
  }
  SB();
  BAR();
  STAMP(17);
  v4u pv[3][8];
#pragma unroll
  for (int i = 0; i < 3; ++i)
#pragma unroll
    for (int k = 0; k < 8; ++k) pv[i][k] = bload<SC1>(rs, dv, OFF_P + (l * 8 + k) * XF_BYTES + db + i * 2048);
  SB();
  float mu = 0.f, m2 = 0.f;
#pragma unroll
  for (int i = 0; i < 3; ++i) mu += (sm[i][0] + sm[i][1]) + (sm[i][2] + sm[i][3]);
  mu *= (1.0f / 12);
#pragma unroll
  for (int i = 0; i < 3; ++i)
#pragma unroll
    for (int e = 0; e < 4; ++e) { const float d = sm[i][e] - mu; m2 += sv[i][e] + 48.f * d * d; }
  const float r = ln_rstd(m2), r2 = r * r;
  float xv[3][8];
  float sum = 0.f;
  unsigned sel_lo = 0x00003f80u, sel_hi = 0x3f800000u;
  asm volatile("" : "+s"(sel_lo), "+s"(sel_hi));
#pragma unroll
  for (int i = 0; i < 3; ++i) {
    float y[8];
#pragma unroll
    for (int e = 0; e < 8; ++e) y[e] = 0.f;
#pragma unroll
    for (int k = 0; k < 8; ++k)
#pragma unroll
      for (int e = 0; e < 4; ++e) { y[2 * e] = dot2(pv[i][k][e], sel_lo, y[2 * e]); y[2 * e + 1] = dot2(pv[i][k][e], sel_hi, y[2 * e + 1]); }
    v4u o;
#pragma unroll
    for (int e = 0; e < 4; ++e) {
      o[e] = pack2(bflo(xr[i][e]) + y[2 * e] * r2, bfhi(xr[i][e]) + y[2 * e + 1] * r2);
      xv[i][2 * e] = bflo(o[e]); xv[i][2 * e + 1] = bfhi(o[e]);
      sum += xv[i][2 * e] + xv[i][2 * e + 1];
    }
    if (LAST) *(v4u*)(out + (size_t)(64 * g + 8 * j + tk) * D_ + (24 * w + 8 * i + gl) * 8) = o;
    else bstore(o, rs, dv, OFF_X + XF_BYTES + db + i * 2048);
  }
  if (LAST) return;
  const float mw = sum_q(add_x8(sum)) * (1.0f / 192);
  float q2 = 0.f;
#pragma unroll
  for (int i = 0; i < 3; ++i)
#pragma unroll
    for (int e = 0; e < 8; ++e) { const float a = xv[i][e] - mw; q2 += a * a; }
  q2 = sum_q(add_x8(q2));
  LAS f32x2* red = (LAS f32x2*)L;
  if (lane < 8) red[w * 8 + lane] = f32x2{mw, q2};
  asm volatile("s_waitcnt lgkmcnt(0)\n\ts_barrier" ::: "memory");
  const f32x2 s0 = red[tk], s1 = red[8 + tk], s2 = red[16 + tk];
  const float mean = (s0[0] + s1[0] + s2[0]) * (1.0f / 3);
  const float d0 = s0[0] - mean, d1 = s1[0] - mean, d2 = s2[0] - mean;
  const float rn = ln_rstd(s0[1] + s1[1] + s2[1] + 192.f * (d0 * d0 + d1 * d1 + d2 * d2));
#pragma unroll
  for (int i = 0; i < 3; ++i) {
    v4u o;
#pragma unroll
    for (int e = 0; e < 4; ++e) o[e] = pack2((xv[i][2 * e] - mean) * rn, (xv[i][2 * e + 1] - mean) * rn);
    bstore(o, rs, dv, OFF_XN + XF_BYTES + db + i * 2048);
  }
  STAMP(18);
  DRAIN();
  BAR();
  STAMP(19);
}

__global__ __launch_bounds__(256, 1) void k_main(unsigned char* __restrict__ ws, u16* __restrict__ out) {
  extern __shared__ __attribute__((aligned(16))) unsigned char smem_[];
  LAS unsigned char* L = (LAS unsigned char*)smem_;
  const unsigned Lb = (unsigned)(uintptr_t)L;
  int w = __builtin_amdgcn_readfirstlane(threadIdx.x >> 6);
  int j = (blockIdx.x >> 3) & 7, g = 4 * (blockIdx.x & 7) + (blockIdx.x >> 6);
  int jc = blockIdx.x & 7, gc = blockIdx.x >> 3;
#define FRESH() asm volatile("" : "+s"(w), "+s"(j), "+s"(g), "+s"(jc), "+s"(gc), "+s"(l))
  const rsrc_t rs = __builtin_amdgcn_make_buffer_rsrc(ws, 0, WS_TOTAL, 0x00020000);

  asm volatile("" ::: "a0", "a255");
  sfor<0, 36>([&](auto i) { wload<i, i>(rs, (threadIdx.x & 63) * 16, OFF_WQ + (4 * j + w) * 72 * 1024); });

#pragma unroll 1
  for (int l = 0; l < 2; ++l) {
    int tid = threadIdx.x;
    asm volatile("" : "+v"(tid));
    const int lane = tid & 63, tok = lane & 15, q = lane >> 4, lane16 = lane * 16;
    FRESH();
    if (l != 0 && w == 3) {
      arrive(ws, 2, gc, lane);
      wait_cnt(ws, 2, g, g, 8u, lane);
      BAR();
    }
    const int wq_b = OFF_WQ + (4 * j + w) * 72 * 1024;
    sfor<36, 64>([&](auto i) { wload<i, i>(rs, lane16, wq_b); });
    if (l != 0) {
      if (w == 3) {
        BAR();
        BAR();
        arrive(ws, 3, g, lane);
        wait_cnt(ws, 3, g, g == 0 ? 0 : g - 1, 8u, lane);
      } else {
        phase_d<false>(L, rs, out, 0, g, j, w, lane);
      }
      BAR();
    }
    FRESH();
    STAMP(0);
    v2u res[3][2];
    const int rv = ((q >> 1) * 16 + tok) * 16 + (q & 1) * 8;
    int wb = OFF_WF + (4 * jc + w) * (RUN_FR * 1024);
    {
      const int xoff = OFF_XN + l * XF_BYTES;
      const int tth = g == 0 ? 0 : 4 * g - 1;
      {
        const unsigned m0 = Lb + (w + 1) * TT_BYTES, so = xoff + (4 * g + w) * TT_BYTES;
        const int hw = w < 3 ? w : 2;
        const unsigned m0h = Lb + hw * 2048, soh = xoff + tth * TT_BYTES + hw * 2048;
#pragma unroll
        for (int c = 0; c < 3; ++c) {
          dma<3>(rs, m0 + c * 6144, lane16, so + c * 6144);
          dma<3>(rs, m0 + c * 6144 + 3072, lane16, so + c * 6144 + 3072);
          dma<2>(rs, m0h + c * 6144, lane16, soh + c * 6144);
        }
      }
      f32x4 acc[4][5];
      LAS unsigned char* lp0 = L + lane16;
      LAS unsigned char* lp1 = L + lane16 + 3 * TT_BYTES;
      asm volatile("" : "+v"(lp1));
#define XA(t, s) (*(LAS v4u*)(((t) < 3 ? lp0 + (t) * TT_BYTES : lp1 + ((t) - 3) * TT_BYTES) + (s) * 1024))
      v4u B[2][5];
      const int wq2 = OFF_WQ + (4 * j + w) * 72 * 1024;
      const int nx1 = w < 3 ? OFF_WO + ((3 * (j & 3) + w) * 54 - 10) * 1024 : wb, nx2 = w < 3 ? nx1 : OOB;
      sfor<0, KS>([&](auto s) {
        if constexpr (s % 6 == 0) {
          wait_vm<s == 0 ? 16 : s == 6 ? 28 : 44>();
          BAR();
          if constexpr (s == 0) STAMP(1);
#pragma unroll
          for (int t = 0; t < 5; ++t) B[s & 1][t] = XA(t, s);
          SB();
        }
        if constexpr (s == 16) wait_vm<52>();
        sfor<0, 4>([&](auto a) {
          sfor<0, 5>([&](auto t) {
            if constexpr (!(a == 3 && t == 0)) mf_av<(4 * s + a) % 64, s == 0>(acc[a][t], B[s & 1][t]);
            if constexpr (a == 0 && (s + 1) % 6 != 0) { B[(s + 1) & 1][t] = XA(t, s + 1); SB(); }
            if constexpr (a >= 1 && a < 3 && (t == 1 || t == 3) && s >= 1 && s <= 16) {
              constexpr int k = 4 * (s - 1) + 2 * (a - 1) + t / 2;
              if constexpr (k < 8) wload<k, 64 + k>(rs, lane16, wq2);
              else wload<k, k>(rs, lane16, k < 10 ? wb : k < 36 ? nx1 : nx2);
            }
          });
        });
      });
      SB();
      STAMP(2);
      FRESH();
      {
        const int rb = w < 3 ? OFF_X + l * XF_BYTES + (4 * g + 2 * (j >> 2)) * TT_BYTES + (9 * (j & 3) + 3 * w) * 512 : OOB;
#pragma unroll
        for (int a = 0; a < 3; ++a)
#pragma unroll
          for (int t = 0; t < 2; ++t) res[a][t] = __builtin_amdgcn_raw_buffer_load_b64(rs, rv + a * 512, rb + t * TT_BYTES, SC1);
        asm volatile("s_nop 3");
        SB();
      }
#define RING4(n) do { SB(); sfor<n, n + 4>([&](auto i) { wload<i, i>(rs, lane16, wb); }); SB(); } while (0)
      LAS unsigned char* E = L + E_OFF + w * (80 * EROW);
#pragma unroll
      for (int t = 0; t < 5; ++t)
#pragma unroll
        for (int a = 0; a < 4; ++a) {
          if (a == 3 && t == 0) continue;
          const f32x4 v = acc[a][t];
          *(LAS v2u*)(E + (16 * t + tok) * EROW + a * 32 + q * 8) = v2u{pack2(v[0], v[1]), pack2(v[2], v[3])};
        }
      RING4(0);
      __builtin_amdgcn_fence(__ATOMIC_RELEASE, "wavefront");
      __builtin_amdgcn_wave_barrier();
      __builtin_amdgcn_fence(__ATOMIC_ACQUIRE, "wavefront");
      LAS unsigned char* er = E + (2 + lane) * EROW;
      LAS unsigned char* et = er + 64 + 4 * (w & 1);
      const int pos = 64 * g + lane;
      const v4u q0 = *(LAS v4u*)(er + 14 * EROW + 96), q1 = *(LAS v4u*)(er + 14 * EROW + 112);
      const unsigned q2 = *(LAS unsigned*)(et + 14 * EROW + 16);
      v4u k0[8], k1[8];
      unsigned kt[8], vt[8];
#pragma unroll
      for (int i = 0; i < 8; ++i) {
        k0[i] = *(LAS v4u*)(er + (14 - 2 * i) * EROW); k1[i] = *(LAS v4u*)(er + (14 - 2 * i) * EROW + 16);
        kt[i] = *(LAS unsigned*)(et + (14 - 2 * i) * EROW); vt[i] = *(LAS unsigned*)(et + (14 - 2 * i) * EROW + 8);
      }
      float lg[8];
#pragma unroll
      for (int i = 0; i < 8; ++i) lg[i] = dot2(q2, kt[i], 0.f);
#pragma unroll
      for (int e = 0; e < 4; ++e)
#pragma unroll
        for (int i = 0; i < 8; ++i) lg[i] = dot2(q0[e], k0[i][e], lg[i]);
#pragma unroll
      for (int e = 0; e < 4; ++e)
#pragma unroll
        for (int i = 0; i < 8; ++i) lg[i] = dot2(q1[e], k1[i][e], lg[i]);
      v4u v0[8], v1[8];
#pragma unroll
      for (int i = 0; i < 8; ++i) { v0[i] = *(LAS v4u*)(er + (14 - 2 * i) * EROW + 32); v1[i] = *(LAS v4u*)(er + (14 - 2 * i) * EROW + 48); }
      RING4(4);
      const float sc = 0.23570226039551584f * 1.4426950408889634f;
#pragma unroll
      for (int i = 1; i < 8; ++i) lg[i] = (pos - 2 * i >= 0) ? lg[i] : -3e38f;
      float mx = lg[0];
#pragma unroll
      for (int i = 1; i < 8; ++i) mx = fmaxf(mx, lg[i]);
      const float nb_ = -mx * sc;
      unsigned pp[4];
      float sum = 0.f;
#pragma unroll
      for (int i = 0; i < 4; ++i) {
        pp[i] = pack2(__builtin_amdgcn_exp2f(fmaf(lg[2 * i], sc, nb_)), __builtin_amdgcn_exp2f(fmaf(lg[2 * i + 1], sc, nb_)));
        sum = dot2(pp[i], 0x3f803f80u, sum);
      }
      const float inv = __builtin_amdgcn_rcpf(sum);
      float o[18];
#pragma unroll
      for (int d = 0; d < 18; ++d) o[d] = 0.f;
#pragma unroll
      for (int i = 0; i < 4; ++i) {
#pragma unroll
        for (int p = 0; p < 9; ++p) {
          const unsigned a = p < 4 ? v0[2 * i][p] : p < 8 ? v1[2 * i][p - 4] : vt[2 * i];
          const unsigned b = p < 4 ? v0[2 * i + 1][p] : p < 8 ? v1[2 * i + 1][p - 4] : vt[2 * i + 1];
          o[2 * p] = dot2(__builtin_amdgcn_perm(b, a, 0x05040100u), pp[i], o[2 * p]);
          o[2 * p + 1] = dot2(__builtin_amdgcn_perm(b, a, 0x07060302u), pp[i], o[2 * p + 1]);
        }
      }
#pragma unroll
      for (int d = 0; d < 18; ++d) o[d] *= inv;
      const int ob = OFF_O + l * XF_BYTES + 4 * g * TT_BYTES + 9 * j * 256, ov = q * TT_BYTES + tok * 16;
      bstore(v4u{pack2(o[0], o[1]), pack2(o[2], o[3]), pack2(o[4], o[5]), pack2(o[6], o[7])}, rs, ov, ob + 2 * w * 256);
      bstore(v4u{pack2(o[8], o[9]), pack2(o[10], o[11]), pack2(o[12], o[13]), pack2(o[14], o[15])}, rs, ov + 256, ob + 2 * w * 256);
      __builtin_amdgcn_raw_buffer_store_b32(pack2(o[16], o[17]), rs, ov, ob + 8 * 256 + 4 * w, SC1);
      STAMP(3);
      DRAIN();
      BAR();
      STAMP(4);
    }
    FRESH();
    if (w == 3) {
      arrive(ws, 0, g, lane);
      wait_cnt(ws, 0, g, g, 8u * (l + 1), lane);
      BAR();
    }
#pragma unroll
    for (int c = 0; c < 6; ++c) dma<3, false>(rs, Lb + TW_OFF + w * TT_BYTES + c * 3072, lane16, wb + (RUN_FR - 18) * 1024 + c * 3072);
    if (w == 3) {
      BAR();
      BAR();
      arrive(ws, 1, g, lane);
      wait_cnt(ws, 1, gc, gc, 8u * (l + 1), lane);
    } else {
      const int nb = j & 3, tt0 = 4 * g + 2 * (j >> 2);
      const unsigned om0 = Lb + w * 12288, oso = OFF_O + l * XF_BYTES + tt0 * TT_BYTES + w * 12288;
      BAR();
      STAMP(5);
#pragma unroll
      for (int c = 0; c < 4; ++c) dma<3>(rs, om0 + c * 3072, lane16, oso + c * 3072);
      DRAIN();
      BAR();
      STAMP(6);
      LAS unsigned char* lp = L + lane16;
      v4u Bo[KS][2];
#pragma unroll
      for (int s = 0; s < 3; ++s) { Bo[s][0] = *(LAS v4u*)(lp + s * 1024); Bo[s][1] = *(LAS v4u*)(lp + TT_BYTES + s * 1024); }
      SB();
      f32x4 acc[3][2];
      sfor<0, KS>([&](auto s) {
        sfor<0, 3>([&](auto a) {
          mf_av<10 + 3 * s + a, s == 0>(acc[a][0], Bo[s][0]);
          if constexpr (a < 2 && s + 3 < KS) { Bo[s + 3][a] = *(LAS v4u*)(lp + a * TT_BYTES + (s + 3) * 1024); SB(); }
          mf_av<10 + 3 * s + a, s == 0>(acc[a][1], Bo[s][1]);
          if constexpr (a >= 1 && s >= 1 && s <= 13) wload<10 + 2 * (s - 1) + a - 1, 10 + 2 * (s - 1) + a - 1>(rs, lane16, wb);
        });
      });
      SB();
      asm volatile("s_nop 7");
      SB();
      const int rb = tt0 * TT_BYTES + (9 * nb + 3 * w) * 512;
      float xv[2][12], sum[2] = {0.f, 0.f};
#pragma unroll
      for (int t = 0; t < 2; ++t)
#pragma unroll
        for (int a = 0; a < 3; ++a) {
          const f32x4 y = acc[a][t];
          v2u o;
          o[0] = pack2(bflo(res[a][t][0]) + y[0], bfhi(res[a][t][0]) + y[1]);
          o[1] = pack2(bflo(res[a][t][1]) + y[2], bfhi(res[a][t][1]) + y[3]);
          __builtin_amdgcn_raw_buffer_store_b64(o, rs, rv + a * 512, OFF_X1 + l * XF_BYTES + rb + t * TT_BYTES, SC1);
          xv[t][4 * a] = bflo(o[0]); xv[t][4 * a + 1] = bfhi(o[0]); xv[t][4 * a + 2] = bflo(o[1]); xv[t][4 * a + 3] = bfhi(o[1]);
          sum[t] = dot2(o[0], 0x3f803f80u, dot2(o[1], 0x3f803f80u, sum[t]));
        }
      float mv[2], m2v[2];
#pragma unroll
      for (int t = 0; t < 2; ++t) mv[t] = sum_q(sum[t]) * (1.0f / 48);
#pragma unroll
      for (int t = 0; t < 2; ++t) {
        float m2 = 0.f;
#pragma unroll
        for (int i = 0; i < 12; ++i) { const float d = xv[t][i] - mv[t]; m2 += d * d; }
        m2v[t] = sum_q(m2);
      }
      const float sa = (q & 1) ? mv[1] : mv[0], sb = (q & 1) ? m2v[1] : m2v[0];
      __builtin_amdgcn_raw_buffer_store_b32(__builtin_bit_cast(unsigned, q < 2 ? sa : sb), rs, (lane & 31) * 48 + (lane >> 5) * (2048 * 48),
                                            OFF_SC + l * SC_BYTES + tt0 * 16 * 48 + (3 * nb + w) * 4, SC1);
      STAMP(7);
      DRAIN();
      BAR();
      STAMP(8);
    }
    FRESH();
    f32x4 cs[18];
#pragma unroll
    for (int a = 0; a < 18; ++a)
      asm volatile("buffer_load_dwordx4 %0, %1, %2, %3 offen offset:%4" : "=v"(cs[a]) : "v"(q * 16), "s"(rs), "s"(OFF_CSG + (4 * jc + w) * 1152), "i"(a * 64) : "memory");
    BAR();
    STAMP(9);
    {
      FRESH();
      unsigned xm0 = Lb + w * XS;
      unsigned xso = OFF_X1 + l * XF_BYTES + (4 * gc + w) * TT_BYTES;
      v3u pm[4];
#pragma unroll
      for (int t = 0; t < 4; ++t)
        asm volatile("buffer_load_dwordx3 %0, %1, %2, %3 offen offset:%4 sc1" : "=v"(pm[t]) : "v"(tok * 48 + q * 12), "s"(rs), "s"(OFF_SC + l * SC_BYTES + 64 * gc * 48), "i"(t * 768) : "memory");
      dma<3>(rs, xm0, lane16, xso);
      dma<3>(rs, xm0 + 3072, lane16, xso + 3072);
      asm volatile("s_waitcnt vmcnt(6)" : "+v"(pm[0]), "+v"(pm[1]), "+v"(pm[2]), "+v"(pm[3]), "+v"(cs[0]), "+v"(cs[1]), "+v"(cs[2]), "+v"(cs[3]), "+v"(cs[4]), "+v"(cs[5]), "+v"(cs[6]),
                   "+v"(cs[7]), "+v"(cs[8]) :: "memory");
      asm volatile("" : "+v"(cs[9]), "+v"(cs[10]), "+v"(cs[11]), "+v"(cs[12]), "+v"(cs[13]), "+v"(cs[14]), "+v"(cs[15]), "+v"(cs[16]), "+v"(cs[17]) :: "memory");
      f32x4 acc[18][4];
      {
        float nm[4];
#pragma unroll
        for (int t = 0; t < 4; ++t) nm[t] = -ln_mean(sum_q(asf(pm[t][0]) + asf(pm[t][1]) + asf(pm[t][2])) * (1.0f / 12));
        sfor<0, 18>([&](auto ai) {
          constexpr int a = (ai + 2) % 18;
          sfor<0, 4>([&](auto t) {
            if constexpr (acc_in_a(a)) quad_write<accq(a, t)>(cs[a] * nm[t]);
            else { acc[a][t] = cs[a] * nm[t]; asm volatile("" : "+v"(acc[a][t])); }
          });
          SB();
        });
      }
      STAMP(10);
      DRAIN();
      BAR();
      LAS unsigned char* lp = L + lane16;
      sfor<0, 4>([&](auto t) { lload<BQ + t, t * XS>(lp); });
      wait_lgkm<0>();
      STAMP(11);
      wb += 36 * 1024;
      xm0 += 6144; xso += 6144;
      auto g1mf = [&](auto a, auto t, auto qa, auto qb) {
        if constexpr (acc_in_a(a)) mf_aaa<accq(a, t), qa, qb>(); else mf_aa<qa, qb>(acc[a][t]);
      };
#define IC(x) std::integral_constant<int, (x)>{}
#pragma unroll 1
      for (int it = 0; it < 8; ++it) {
        sfor<0, 36>([&](auto i) {
          constexpr int a = i % 18, sec = i / 18;
          wait_vm<i == 0 ? 35 : 34>();
          sfor<0, 4>([&](auto t) {
            g1mf(IC(a), t, i, IC(BQ + 4 * sec + t));
            if constexpr (t == 0 && i > 0) wload<i - 1, i - 1>(rs, lane16, wb);
            if constexpr (t == 1 && a >= 2 && a < 6) lload<BQ + 4 * (1 - sec) + a - 2, (a - 2) * XS + 1024 * (1 + sec)>(lp);
            if constexpr (t == 1 && a == 12) wait_lgkm<0>();
            if constexpr (t == 1 && i == 18) BAR();
            if constexpr (t == 1 && i == 27) dma2_if_lt6(it, rs, xm0, lane16, xso);
          });
        });
        wload<35, 35>(rs, lane16, wb);
        lp += 2048;
        wb += 36 * 1024;
        xm0 += 2048; xso += 2048;
      }
      STAMP(12);
      {
        unsigned hreg[18][4];
        LAS unsigned char* hp = L + lane16 + 4 * w * 1024;
        sfor<0, 36>([&](auto gi) {
          constexpr int a = (gi / 4) * 2 + (gi & 1), sec = (gi >> 1) & 1;
          constexpr int ep = gi / 4 - 1, et = gi & 3;
          wait_vm<gi == 0 ? 35 : 34>();
          if constexpr (gi == 2) wait_lgkm<0>();
          float x, y;
          sfor<0, 4>([&](auto t) {
            g1mf(IC(a), t, gi, IC(BQ + 4 * sec + t));
            if constexpr (gi == 0) lload<BQ + 4 + t, t * XS + 1024>(lp);
            if constexpr (t == 0 && gi > 0) wload<gi - 1, gi - 1>(rs, lane16, wb);
            if constexpr (ep >= 0) {
              constexpr int e = t & 2, d = 2 * ep + t / 2;
              if constexpr ((t & 1) == 0) {
                if constexpr (acc_in_a(2 * ep)) hp1_a<accq(2 * ep, et) * 4 + e>(x, y); else hp1(x, y, acc[2 * ep][et][e], acc[2 * ep][et][e + 1]);
              } else {
                if constexpr (acc_in_a(2 * ep)) hreg[d][et] = hp2_a<accq(2 * ep + 1, et) * 4 + e>(x, y);
                else hreg[d][et] = hp2(x, y, acc[2 * ep + 1][et][e], acc[2 * ep + 1][et][e + 1]);
                if constexpr ((d & 3) == 3) {
                  *(LAS v4u*)(hp + et * XS + (d >> 2) * 1024) = v4u{hreg[d - 3][et], hreg[d - 2][et], hreg[d - 1][et], hreg[d][et]};
                  SB();
                }
              }
            }
          });
        });
        wload<35, 35>(rs, lane16, wb);
        LAS unsigned char* hq = L + lane16 + (18 + (w >> 1)) * 1024 + (w & 1) * 8;
#pragma unroll
        for (int t = 0; t < 4; ++t) {
          const unsigned h0 = hpair(acc[16][t][0], acc[16][t][1], acc[17][t][0], acc[17][t][1]);
          const unsigned h1 = hpair(acc[16][t][2], acc[16][t][3], acc[17][t][2], acc[17][t][3]);
          *(LAS v2u*)(hq + t * XS) = v2u{h0, h1};
          SB();
        }
      }
      STAMP(13);
      asm volatile("s_waitcnt lgkmcnt(0)\n\ts_barrier" ::: "memory");
      wb += 36 * 1024;
      LAS unsigned char* hl0 = L + lane16;
      LAS unsigned char* hl1 = L + 2 * XS + lane16;
      asm volatile("" : "+v"(hl1));
      constexpr int HV = 11;
#define HOFF(s, t) (((t) & 1) * XS + ((s) < 16 ? (s) : (s) + 2) * 1024)
#define HPTR(t) ((t) < 2 ? hl0 : hl1)
      v4u Hf[HV][4];
#pragma unroll
      for (int s = 0; s < 2; ++s)
#pragma unroll
        for (int t = 0; t < 4; ++t) Hf[s][t] = *(LAS v4u*)(HPTR(t) + HOFF(s, t));
      SB();
      STAMP(14);
      FRESH();
      const int s0 = (9 * w + (w & 1)) >> 1;
      const int T8 = (w & 1) ? 9 * w : 9 * w + 8;
      int pso = OFF_P + (l * 8 + jc) * XF_BYTES + 4 * gc * TT_BYTES;
      const int pso8 = pso + (T8 >> 1) * 1024 + (T8 & 1) * 8;
      pso += s0 * 1024;
      f32x4 c0[4], c1[4];
      v4u pk[4];
      auto pair_iter = [&](auto first, int nb_) {
        sfor<0, 36>([&](auto i) {
          constexpr int s = i / 2;
          constexpr int nst = first ? 0 : (i <= 4 ? 0 : i <= 10 ? 1 : i <= 16 ? 2 : i <= 22 ? 3 : 4);
          wait_vm<(i == 0 ? 35 : 34) + nst>();
          if constexpr (first && s >= HV && (i & 1) == 0) wait_lgkm<(s + 1 < KS ? 4 : 0)>();
          sfor<0, 4>([&](auto t) {
            f32x4& c = (i & 1) ? c1[t] : c0[t];
            if constexpr (!first && s == 0) { pk[t][2 * i] = pack2(c[0], c[1]); pk[t][2 * i + 1] = pack2(c[2], c[3]); SB(); }
            if constexpr (s < HV) mf_av<i, s == 0>(c, Hf[s][t]); else mf_aa<i, 36 + (s - HV) * 4 + t>(c);
            if constexpr (t == 0 && i > 0) wload<i - 1, i - 1>(rs, lane16, nb_);
            if constexpr (first && s + 2 < KS && t >= 1 && t < 3) {
              constexpr int ht = (i & 1) * 2 + t - 1;
              if constexpr (s + 2 < HV) { Hf[s + 2][ht] = *(LAS v4u*)(HPTR(ht) + HOFF(s + 2, ht)); SB(); }
              else lload<36 + (s + 2 - HV) * 4 + ht, HOFF(s + 2, ht)>(HPTR(ht));
            }
            if constexpr (!first && t == 2 && i >= 4 && i <= 22 && (i - 4) % 6 == 0) {
              __builtin_amdgcn_raw_buffer_store_b128(pk[(i - 4) / 6], rs, lane16, pso + ((i - 4) / 6) * TT_BYTES, SC1);
              SB();
            }
          });
        });
        wload<35, 35>(rs, lane16, nb_);
        wb += 36 * 1024;
        if constexpr (!first) pso += 1024;
      };
      pair_iter(std::true_type{}, wb);
      STAMP(20);
      const int wnext = l == 0 ? OFF_WQ + (4 * j + w) * 72 * 1024 : OOB;
#pragma unroll 1
      for (int p = 1; p < 4; ++p) { pair_iter(std::false_type{}, p < 3 ? wb : wnext); STAMP(20 + p); }
      {
        LAS unsigned char* tw = L + TW_OFF + w * TT_BYTES + lane16;
        v4u At[KS];
#pragma unroll
        for (int s = 0; s < 3; ++s) At[s] = *(LAS v4u*)(tw + s * 1024);
        SB();
#pragma unroll
        for (int t = 0; t < 4; ++t) { pk[t][0] = pack2(c0[t][0], c0[t][1]); pk[t][1] = pack2(c0[t][2], c0[t][3]); }
        SB();
        sfor<0, KS>([&](auto s) {
          sfor<0, 4>([&](auto t) {
            if constexpr (s < HV) mf_vv<s == 0>(c0[t], At[s], Hf[s][t]); else mf_va<36 + (s - HV) * 4 + t>(c0[t], At[s]);
            if constexpr (s == 0) { pk[t][2] = pack2(c1[t][0], c1[t][1]); pk[t][3] = pack2(c1[t][2], c1[t][3]); SB(); }
            if constexpr (t == 1 && s + 3 < KS) { At[s + 3] = *(LAS v4u*)(tw + (s + 3) * 1024); SB(); }
            if constexpr (t == 2 && s >= 1 && s <= 4) { __builtin_amdgcn_raw_buffer_store_b128(pk[s - 1], rs, lane16, pso + (s - 1) * TT_BYTES, SC1); SB(); }
          });
        });
        asm volatile("s_nop 7");
        SB();
#pragma unroll
        for (int t = 0; t < 4; ++t) {
          const f32x4 u8 = c0[t];
          __builtin_amdgcn_raw_buffer_store_b64(v2u{pack2(u8[0], u8[1]), pack2(u8[2], u8[3])}, rs, lane16, pso8 + t * TT_BYTES, SC1);
        }
      }
      STAMP(15);
      DRAIN();
      BAR();
      STAMP(16);
    }
  }
  {
    const int l = 1, lane = threadIdx.x & 63;
    if (w == 3) {
      arrive(ws, 2, gc, lane);
      wait_cnt(ws, 2, g, g, 16u, lane);
      BAR();
    } else {
      phase_d<true>(L, rs, out, l, g, j, w, lane);
    }
  }
}

extern "C" { size_t g_offs[16] = {OFF_WQ, OFF_WO, OFF_WF, 0, OFF_CSG, OFF_XN, OFF_SC, OFF_TMO, OFF_X, OFF_X1, OFF_O, OFF_P, WS_TOTAL}; }
extern "C" void kernel_launch(void* const* d_in, const int* in_sizes, int n_in, void* d_out, int out_size, void* d_ws,
                              size_t ws_size, hipStream_t stream) {
  const u16* x = (const u16*)d_in[0];
  const u16* wqkv = (const u16*)d_in[1];
  const u16* wo = (const u16*)d_in[2];
  const u16* wg = (const u16*)d_in[4];
  const u16* wu = (const u16*)d_in[5];
  const u16* wd = (const u16*)d_in[6];
  unsigned char* ws = (unsigned char*)d_ws;
  (void)hipFuncSetAttribute((const void*)k_prep, hipFuncAttributeMaxDynamicSharedMemorySize, PREP_LDS);
  (void)hipFuncSetAttribute((const void*)k_main, hipFuncAttributeMaxDynamicSharedMemorySize, MAIN_LDS);
  k_prep<<<PREP_UNITS, 256, PREP_LDS, stream>>>(x, wqkv, wo, wg, wu, wd, ws);
  k_main<<<256, 256, MAIN_LDS, stream>>>(ws, (u16*)d_out);
}
```

```cpp
#include <hip/hip_runtime.h>
#include <hip/hip_bf16.h>
#include <stdint.h>
#include <type_traits>

#define LAS __attribute__((address_space(3)))
typedef unsigned v4u __attribute__((ext_vector_type(4)));
typedef unsigned v2u __attribute__((ext_vector_type(2)));
typedef float f32x4 __attribute__((ext_vector_type(4)));
typedef float f32x2 __attribute__((ext_vector_type(2)));
typedef __bf16 bf16x2 __attribute__((ext_vector_type(2)));
typedef unsigned short u16;
typedef __amdgpu_buffer_rsrc_t rsrc_t;

constexpr int S_ = 2048, D_ = 576, F_ = 4608;
constexpr int KS = 18;
constexpr int XF_BYTES = S_ * D_ * 2;
constexpr int TT_BYTES = KS * 1024;
constexpr int RUN_FR = 486;

constexpr int SS = 128;
constexpr int OFF_EP = 0, OFF_TMO = 128;
constexpr int OFF_FQ = 4096;
constexpr int OFF_FF = OFF_FQ + 10 * 4096;
constexpr int OFF_FG = OFF_FF + 8 * 4096;
constexpr int OFF_WQ = 524288;
constexpr int OFF_WO = OFF_WQ + 32 * 72 * 1024;
constexpr int OFF_WF = OFF_WO + 36 * 18 * 1024;
constexpr int OFF_CSG = OFF_WF + 32 * RUN_FR * 1024;
constexpr int OFF_SC = OFF_CSG + 32 * 18 * 16 * 4;
constexpr int OFF_X = OFF_SC + 64 * 12 * 256;
constexpr int OFF_XN = OFF_X + XF_BYTES;
constexpr int OFF_X1 = OFF_XN + 2 * XF_BYTES;
constexpr int OFF_O = OFF_X1 + XF_BYTES;
constexpr int OFF_P = OFF_O + XF_BYTES;
constexpr int WS_TOTAL = OFF_P + 8 * XF_BYTES;
static_assert(OFF_FG + 10 * 32 * 8 * SS <= OFF_WQ, "flags");
static_assert(OFF_X % 256 == 0 && OFF_SC % 256 == 0, "alignment");
constexpr int OOB = 0x70000000;

constexpr int SC1 = 16;

#define SB() __builtin_amdgcn_sched_barrier(0)
#define BAR() asm volatile("s_barrier" ::: "memory")
#define DRAIN() do { __builtin_amdgcn_s_waitcnt(0x0F70); asm volatile("s_waitcnt vmcnt(0)" ::: "memory"); } while (0)

__device__ __forceinline__ unsigned pack2(float a, float b) {
  f32x2 v = {a, b};
  return __builtin_bit_cast(unsigned, __builtin_convertvector(v, bf16x2));
}
__device__ __forceinline__ float asf(unsigned u) { return __builtin_bit_cast(float, u); }
__device__ __forceinline__ float bflo(unsigned u) { return __builtin_bit_cast(float, u << 16); }
__device__ __forceinline__ float bfhi(unsigned u) { return __builtin_bit_cast(float, u & 0xffff0000u); }
__device__ __forceinline__ float rbf(float a) { return bflo(pack2(a, a)); }
__device__ __forceinline__ float ln_rstd(float m2) { return __builtin_amdgcn_rcpf(rbf(__builtin_amdgcn_sqrtf(rbf(rbf(m2 * (1.0f / D_)) + 1e-5f)))); }
__device__ __forceinline__ float sum_q(float x) {
  unsigned u = __builtin_bit_cast(unsigned, x);
  auto r = __builtin_amdgcn_permlane16_swap(u, u, false, false);
  x = asf(r[0]) + asf(r[1]);
  u = __builtin_bit_cast(unsigned, x);
  auto s = __builtin_amdgcn_permlane32_swap(u, u, false, false);
  return asf(s[0]) + asf(s[1]);
}
__device__ __forceinline__ float add_x8(float x) {
  return x + __builtin_bit_cast(float, __builtin_amdgcn_update_dpp(0, __builtin_bit_cast(int, x), 0x128, 0xf, 0xf, false));
}
__device__ __forceinline__ float dot2(unsigned a, unsigned b, float c) {
  return __builtin_amdgcn_fdot2_f32_bf16(__builtin_bit_cast(bf16x2, a), __builtin_bit_cast(bf16x2, b), c, false);
}
__device__ __forceinline__ float max3(float a, float b, float c) { return __builtin_elementwise_maximum(__builtin_elementwise_maximum(a, b), c); }
template <int I, int N, class Fn> __device__ __forceinline__ void sfor(Fn&& fn) {
  if constexpr (I < N) { fn(std::integral_constant<int, I>{}); sfor<I + 1, N>(fn); }
}

typedef short v4s __attribute__((ext_vector_type(4)));
__device__ __forceinline__ v4u tr_frag(LAS unsigned char* p0, LAS unsigned char* p1) {
  const v2u lo = __builtin_bit_cast(v2u, __builtin_amdgcn_ds_read_tr16_b64_v4i16((LAS v4s*)p0));
  const v2u hi = __builtin_bit_cast(v2u, __builtin_amdgcn_ds_read_tr16_b64_v4i16((LAS v4s*)p1));
  return v4u{lo[0], lo[1], hi[0], hi[1]};
}
template <int AUX> __device__ __forceinline__ v4u bload(rsrc_t rs, int voff, int soff) { return __builtin_amdgcn_raw_buffer_load_b128(rs, voff, soff, AUX); }
__device__ __forceinline__ void bstore(v4u v, rsrc_t rs, int voff, int soff) {
  __builtin_amdgcn_raw_buffer_store_b128(v, rs, voff, soff, SC1);
  asm volatile("s_nop 1" :: "v"(v) : "memory");
}
#define AQ(n) "a[%c" #n "*4:%c" #n "*4+3]"
template <int Q, int F> __device__ __forceinline__ void wload(rsrc_t rs, int lane16, int base) {
  asm volatile("buffer_load_dwordx4 " AQ(0) ", %1, %2, %3 offen offset:%4 sc1" :: "i"(Q), "v"(lane16), "s"(rs), "s"(base + (F >> 2) * 4096), "i"((F & 3) * 1024) : "memory");
}
template <int Q, int OFF> __device__ __forceinline__ void lload(LAS unsigned char* p) {
  asm volatile("ds_read_b128 " AQ(0) ", %1 offset:%2" :: "i"(Q), "v"(p), "i"(OFF) : "memory");
}
template <int N> __device__ __forceinline__ void wait_vm() { asm volatile("s_waitcnt vmcnt(%0)" :: "i"(N) : "memory"); }
template <int N> __device__ __forceinline__ void wait_lgkm() { asm volatile("s_waitcnt lgkmcnt(%0)" :: "i"(N) : "memory"); }
#define DMA1(o, c) "buffer_load_dwordx4 %1, %2, %3 offen offset:" #o c " lds\n\t"
template <int N> __device__ __forceinline__ void dma(rsrc_t rs, unsigned m0v, int voff, unsigned soff) {
  if constexpr (N == 2) asm volatile("s_mov_b32 m0, %0\n\ts_nop 0\n\t" DMA1(0, " sc1") DMA1(1024, " sc1") :: "s"(m0v), "v"(voff), "s"(rs), "s"(soff) : "memory");
  if constexpr (N == 3) asm volatile("s_mov_b32 m0, %0\n\ts_nop 0\n\t" DMA1(0, " sc1") DMA1(1024, " sc1") DMA1(2048, " sc1") :: "s"(m0v), "v"(voff), "s"(rs), "s"(soff) : "memory");
}
__device__ __forceinline__ void dma2_if_lt6(int it, rsrc_t rs, unsigned m0v, int voff, unsigned soff) {
  asm volatile("s_cmp_ge_u32 %4, 6\n\ts_cbranch_scc1 .Lskip%=\n\ts_mov_b32 m0, %0\n\ts_nop 0\n\t" DMA1(0, " sc1") DMA1(1024, " sc1") ".Lskip%=:"
               :: "s"(m0v), "v"(voff), "s"(rs), "s"(soff), "s"(it) : "memory", "scc");
}
#define MFS "v_mfma_f32_16x16x32_bf16 "
template <int QA, bool FIRST = false> __device__ __forceinline__ void mf_av(f32x4& c, v4u b) {
  if constexpr (FIRST) asm volatile(MFS "%0, " AQ(1) ", %2, 0" : "=&v"(c) : "i"(QA), "v"(b));
  else asm volatile(MFS "%0, " AQ(1) ", %2, %0" : "+v"(c) : "i"(QA), "v"(b));
}
template <int QA, int QB> __device__ __forceinline__ void mf_aa(f32x4& c) { asm volatile(MFS "%0, " AQ(1) ", " AQ(2) ", %0" : "+v"(c) : "i"(QA), "i"(QB)); }
template <int QC, int QA, int QB> __device__ __forceinline__ void mf_aaa() { asm volatile(MFS AQ(0) ", " AQ(1) ", " AQ(2) ", " AQ(0) :: "i"(QC), "i"(QA), "i"(QB)); }
template <int QB> __device__ __forceinline__ void mf_va(f32x4& c, v4u a) { asm volatile(MFS "%0, %2, " AQ(1) ", %0" : "+v"(c) : "i"(QB), "v"(a)); }
template <bool FIRST = false> __device__ __forceinline__ void mf_vv(f32x4& c, v4u a, v4u b) {
  if constexpr (FIRST) asm volatile(MFS "%0, %1, %2, 0" : "=&v"(c) : "v"(a), "v"(b));
  else asm volatile(MFS "%0, %1, %2, %0" : "+v"(c) : "v"(a), "v"(b));
}
template <int Q> __device__ __forceinline__ void quad_write(f32x4 v) {
  asm volatile("v_accvgpr_write_b32 a[%c0*4], %1\n\tv_accvgpr_write_b32 a[%c0*4+1], %2\n\tv_accvgpr_write_b32 a[%c0*4+2], %3\n\tv_accvgpr_write_b32 a[%c0*4+3], %4"
               :: "i"(Q), "v"(v[0]), "v"(v[1]), "v"(v[2]), "v"(v[3]));
}
constexpr bool acc_in_a(int a) { return a >= 2 && a < 6; }
constexpr int accq(int a, int t) { return 36 + (a - 2) * 4 + t; }
constexpr int BQ = 52;
__device__ __forceinline__ unsigned hpair(float g0, float g1, float u0, float u1) {
  unsigned h; float t;
  asm volatile("v_max_f32 %0, 0, %2\n\tv_max_f32 %1, 0, %3\n\tv_mul_f32 %0, %0, %4\n\tv_mul_f32 %1, %1, %5\n\tv_cvt_pk_bf16_f32 %0, %0, %1"
               : "=&v"(h), "=&v"(t) : "v"(g0), "v"(g1), "v"(u0), "v"(u1));
  return h;
}
#define G_MF(t) MFS "%[c" #t "], a[%c[qa]*4:%c[qa]*4+3], a[%c[qb]*4+4*" #t ":%c[qb]*4+4*" #t "+3], %[c" #t "]\n\t"
#define G_MA(t) MFS "a[%c[qc]*4+4*" #t ":%c[qc]*4+4*" #t "+3], a[%c[qa]*4:%c[qa]*4+3], a[%c[qb]*4+4*" #t ":%c[qb]*4+4*" #t "+3], a[%c[qc]*4+4*" #t ":%c[qc]*4+4*" #t "+3]\n\t"
#define G_LD "buffer_load_dwordx4 a[%c[qa]*4-4:%c[qa]*4-1], %[vo], %[rs], %[so] offen offset:%[io] sc1\n\t"
#define G_V(M) M(0) "v_max_f32 %[h0], 0, %[g0]\n\tv_max_f32 %[x], 0, %[g1]\n\t" G_LD M(1) "v_mul_f32 %[h0], %[h0], %[u0]\n\tv_mul_f32 %[x], %[x], %[u1]\n\tv_max_f32 %[h1], 0, %[g2]\n\t" \
               M(2) "v_cvt_pk_bf16_f32 %[h0], %[h0], %[x]\n\tv_max_f32 %[x], 0, %[g3]\n\tv_mul_f32 %[h1], %[h1], %[u2]\n\t" M(3) "v_mul_f32 %[x], %[x], %[u3]\n\tv_cvt_pk_bf16_f32 %[h1], %[h1], %[x]"
#define G_RD(d, o) "v_accvgpr_read_b32 %[" #d "], a[%c[rg]+" #o "]\n\t"
#define G_A(M) M(0) G_RD(h0, 0) G_RD(x, 1) G_RD(y, 16) "v_max_f32 %[h0], 0, %[h0]\n\t" G_LD M(1) "v_max_f32 %[x], 0, %[x]\n\tv_mul_f32 %[h0], %[h0], %[y]\n\t" G_RD(y, 17) G_RD(h1, 2) \
               "v_mul_f32 %[x], %[x], %[y]\n\t" M(2) G_RD(y, 18) "v_cvt_pk_bf16_f32 %[h0], %[h0], %[x]\n\t" G_RD(x, 3) "v_max_f32 %[h1], 0, %[h1]\n\tv_mul_f32 %[h1], %[h1], %[y]\n\t" \
               M(3) G_RD(y, 19) "v_max_f32 %[x], 0, %[x]\n\tv_mul_f32 %[x], %[x], %[y]\n\tv_cvt_pk_bf16_f32 %[h1], %[h1], %[x]"
#define G_C [c0] "+v"(c[0]), [c1] "+v"(c[1]), [c2] "+v"(c[2]), [c3] "+v"(c[3]),
#define G_IN [qa] "i"(QA), [qb] "i"(QB), [vo] "v"(lane16), [rs] "s"(rs), [so] "s"(base + ((QA - 1) >> 2) * 4096), [io] "i"(((QA - 1) & 3) * 1024)
#define G_GU [g0] "v"(g[0]), [g1] "v"(g[1]), [g2] "v"(g[2]), [g3] "v"(g[3]), [u0] "v"(u[0]), [u1] "v"(u[1]), [u2] "v"(u[2]), [u3] "v"(u[3])
template <int QA, int QB, int QC, int RG>
__device__ __forceinline__ void group_h(f32x4 (&c)[4], f32x4 g, f32x4 u, unsigned& h0, unsigned& h1, rsrc_t rs, int lane16, int base) {
  float x, y;
  if constexpr (QC < 0 && RG < 0) asm volatile(G_V(G_MF) : G_C [h0] "=&v"(h0), [h1] "=&v"(h1), [x] "=&v"(x) : G_IN, G_GU : "memory");
  if constexpr (QC >= 0 && RG < 0) asm volatile(G_V(G_MA) : [h0] "=&v"(h0), [h1] "=&v"(h1), [x] "=&v"(x) : G_IN, G_GU, [qc] "i"(QC) : "memory");
  if constexpr (QC < 0 && RG >= 0) asm volatile(G_A(G_MF) : G_C [h0] "=&v"(h0), [h1] "=&v"(h1), [x] "=&v"(x), [y] "=&v"(y) : G_IN, [rg] "i"(RG) : "memory");
  if constexpr (QC >= 0 && RG >= 0) asm volatile(G_A(G_MA) : [h0] "=&v"(h0), [h1] "=&v"(h1), [x] "=&v"(x), [y] "=&v"(y) : G_IN, [rg] "i"(RG), [qc] "i"(QC) : "memory");
}
typedef unsigned long long u64;
constexpr int fg_off(int h, int g) { return OFF_FG + (h * 32 + g) * 8 * SS; }
__device__ __forceinline__ void publish(rsrc_t rs, int off, u64 tag, int lane) {
  __builtin_amdgcn_raw_buffer_store_b64(v2u{(unsigned)tag, (unsigned)(tag >> 32)}, rs, lane == 0 ? 0 : OOB, off, SC1);
}
template <bool AT_ONCE = false>
__device__ __forceinline__ void wait_slots(rsrc_t rs, int off, u64 tag, int lane) {
  if (!AT_ONCE) __builtin_amdgcn_s_sleep(5);
#pragma nounroll
  for (unsigned spins = 1;; ++spins) {
    v2u v;
    asm volatile("buffer_load_dwordx2 %0, %1, %2, 0 offen sc1\n\ts_waitcnt vmcnt(0)" : "=v"(v) : "v"((spins & 63u) ? off : OFF_TMO), "s"(rs) : "memory");
    if (__builtin_amdgcn_ballot_w64(v[0] == (unsigned)tag && v[1] == (unsigned)(tag >> 32)) == ~0ull) break;
    __builtin_amdgcn_s_sleep(1);
    if (spins > (1u << 18)) { publish(rs, OFF_TMO, tag, lane); break; }
  }
}
__device__ __forceinline__ void peek_slots(rsrc_t rs, int voff) {
  asm volatile("buffer_load_dwordx2 v[252:253], %0, %1, 0 offen sc1" :: "v"(voff), "s"(rs) : "v252", "v253", "memory");
}
__device__ __forceinline__ void wait_all_started(rsrc_t rs, u64 tag, int lane) {
#pragma nounroll
  for (unsigned spins = 0; spins < (1u << 18); ++spins) {
    v2u a, b, c, d;
    asm volatile("buffer_load_dwordx2 %0, %4, %5, 0 offen sc1\n\tbuffer_load_dwordx2 %1, %4, %5, 0 offen offset:%6 sc1\n\t"
                 "buffer_load_dwordx2 %2, %4, %5, 0 offen offset:2*%6 sc1\n\tbuffer_load_dwordx2 %3, %4, %5, 0 offen offset:3*%6 sc1\n\ts_waitcnt vmcnt(0)"
                 : "=&v"(a), "=&v"(b), "=&v"(c), "=&v"(d) : "v"(fg_off(7, lane >> 1) + (lane & 1) * 4 * SS), "s"(rs), "i"(SS) : "memory");
    const unsigned lo = (unsigned)tag, hi = (unsigned)(tag >> 32);
    const bool ok = a[0] == lo && a[1] == hi && b[0] == lo && b[1] == hi && c[0] == lo && c[1] == hi && d[0] == lo && d[1] == hi;
    if (__builtin_amdgcn_ballot_w64(ok) == ~0ull) break;
  }
}

constexpr int MAIN_LDS = 157 * 1024;
constexpr int EROW = 112;
constexpr int E_OFF = 0;
constexpr int XS = 20 * 1024;
constexpr int TW_OFF = 4 * XS;
constexpr int CS_OFF = 152 * 1024;
constexpr int DW = 12, RB = 14;
constexpr int RED_OFF = 32 * 1024;


__device__ __forceinline__ void ln_rows(LAS unsigned char* L, rsrc_t rs, const v4u (&xo)[3], float sum, float sq, int ob, const int (&vo)[3], int w, int lane) {
  const int tk = lane & 7;
  sum = sum_q(add_x8(sum));
  sq = sum_q(add_x8(sq));
  LAS f32x2* red = (LAS f32x2*)(L + RED_OFF);
  if (lane < 8) red[w * 8 + lane] = f32x2{sum, sq};
  asm volatile("s_waitcnt lgkmcnt(0)\n\ts_barrier" ::: "memory");
  const f32x2 s0 = red[tk], s1 = red[8 + tk], s2 = red[16 + tk];
  const float mean = (s0[0] + s1[0] + s2[0]) * (1.0f / D_);
  const float rn = ln_rstd(fmaxf(s0[1] + s1[1] + s2[1] - D_ * mean * mean, 0.f)), nb = -rbf(mean) * rn;
#pragma unroll
  for (int i = 0; i < 3; ++i) {
    v4u o;
#pragma unroll
    for (int e = 0; e < 4; ++e) o[e] = pack2(fmaf(bflo(xo[i][e]), rn, nb), fmaf(bfhi(xo[i][e]), rn, nb));
    bstore(o, rs, vo[i], ob);
  }
}

template <bool LAST>
__device__ __forceinline__ void phase_d(LAS unsigned char* L, rsrc_t rs, u16* out, u64 tag, int l, int g, int j, int w, int lane) {
  const int tk = lane & 7, gl = lane >> 3;
  const int db = (4 * g + (j >> 1)) * TT_BYTES + (j & 1) * 128;
  int cg[3], vo[3];
#pragma unroll
  for (int i = 0; i < 2; ++i) { const int e = 4 * w + 2 * i + (gl >> 2); cg[i] = e + (e >= 3 ? 2 : 0) + (e >= 6 ? 1 : 0) + (e >= 9 ? 2 : 0); }
  { const int k = 2 * w + (gl >> 2); cg[2] = k < 2 ? 3 + k : k == 2 ? 8 : k < 5 ? 9 + k : 17; }
#pragma unroll
  for (int i = 0; i < 3; ++i) { cg[i] = 4 * cg[i] + (gl & 3); vo[i] = cg[i] * 256 + tk * 16; }
  {
    u64 ok;
    asm volatile("v_cmp_eq_u64 %0, %1, v[252:253]\n\ts_nop 4" : "=s"(ok) : "s"(tag) : "memory");
    if (ok != ~0ull) wait_slots<true>(rs, fg_off(8 + l, g) + (lane & 7) * SS, tag, lane);
  }
  v4u pv[3][8];
#pragma unroll
  for (int i = 0; i < 2; ++i)
#pragma unroll
    for (int k = 0; k < 8; ++k) pv[i][k] = bload<SC1>(rs, vo[i], OFF_P + k * XF_BYTES + db);
  v4u xr[3];
#pragma unroll
  for (int i = 0; i < 3; ++i) xr[i] = bload<SC1>(rs, vo[i], OFF_X1 + db);
  const int sv = gl * 256 + (8 * (j & 1) + tk) * 16, sb = OFF_SC + (2 * g + (j >> 2)) * 3072 + ((j >> 1) & 1) * 8;
  const v2u st0 = __builtin_amdgcn_raw_buffer_load_b64(rs, sv, sb, SC1), st1 = __builtin_amdgcn_raw_buffer_load_b64(rs, gl < 4 ? sv + 2048 : OOB, sb, SC1);
  SB();
  BAR();
#pragma unroll
  for (int k = 0; k < 8; ++k) pv[2][k] = bload<SC1>(rs, vo[2], OFF_P + k * XF_BYTES + db);
  SB();
  const float mu = sum_q(add_x8(asf(st0[0]) + asf(st1[0]))) * (1.0f / D_);
  const float r = ln_rstd(fmaxf(sum_q(add_x8(asf(st0[1]) + asf(st1[1]))) - D_ * mu * mu, 0.f)), r2 = r * r;
  v4u xo[3];
  float sum = 0.f, sq = 0.f;
  unsigned sel_lo = 0x00003f80u, sel_hi = 0x3f800000u;
  asm volatile("" : "+s"(sel_lo), "+s"(sel_hi));
#pragma unroll
  for (int i = 0; i < 3; ++i) {
    float y[8];
#pragma unroll
    for (int e = 0; e < 8; ++e) y[e] = 0.f;
#pragma unroll
    for (int k = 0; k < 8; ++k)
#pragma unroll
      for (int e = 0; e < 4; ++e) { y[2 * e] = dot2(pv[i][k][e], sel_lo, y[2 * e]); y[2 * e + 1] = dot2(pv[i][k][e], sel_hi, y[2 * e + 1]); }
    v4u o;
#pragma unroll
    for (int e = 0; e < 4; ++e) {
      o[e] = pack2(bflo(xr[i][e]) + y[2 * e] * r2, bfhi(xr[i][e]) + y[2 * e + 1] * r2);
      sum = dot2(o[e], 0x3f803f80u, sum);
      sq = dot2(o[e], o[e], sq);
    }
    xo[i] = o;
    if (LAST) bstore(o, __builtin_amdgcn_make_buffer_rsrc(out, 0, XF_BYTES, 0x00020000), tk * (D_ * 2) + cg[i] * 16, (64 * g + 8 * j) * (D_ * 2));
    else bstore(o, rs, vo[i], OFF_X + db);
    SB();
  }
  if (LAST) return;
  ln_rows(L, rs, xo, sum, sq, OFF_XN + XF_BYTES + db, vo, w, lane);
  sfor<0, DW>([&](auto i) { wload<i, i>(rs, lane * 16, OFF_WQ + (4 * j + w) * 72 * 1024); });
  wait_vm<DW>();
  BAR();
}

__device__ __forceinline__ void touch(rsrc_t rs, int voff, int soff) {
  asm volatile("buffer_load_dword v255, %0, %1, %2 offen sc1" :: "v"(voff), "s"(rs), "s"(soff) : "v255", "memory");
}
__device__ __forceinline__ void code_touch(u64 pc, int len, int gc, int lane) {
  const rsrc_t rc = __builtin_amdgcn_make_buffer_rsrc((void*)pc, 0, len, 0x00020000);
  asm volatile("buffer_load_dword v255, %0, %1, 0 offen" :: "v"((gc + 32 * lane) * 128), "s"(rc) : "v255", "memory");
}
__device__ __forceinline__ void tail_dma(rsrc_t rs, unsigned Lb, int jc, int lane16, int c0, int c1) {
#pragma unroll 1
  for (int c = c0; c < c1; ++c) dma<3>(rs, Lb + TW_OFF + c * 3072, lane16, OFF_WF + ((4 * jc + c / 6) * RUN_FR + RUN_FR - 18) * 1024 + (c % 6) * 3072);
}

constexpr int QPRE0 = 12, QPRE1 = 48;
constexpr int QWB0 = 24, QWB1 = 45;
constexpr int qop(bool l0, int s, int k) {
  if (l0) {
    if (s == 1 || s == 6) return k < 3 ? 100 + 3 * (s == 1 ? 1 : 2) + k : -1;
    const int i = 8 * (s - (s > 1) - (s > 6)) + k + QPRE0;
    if (i < 72) return i;
    if (s >= 10 && k < 3) return 300 + 3 * (s - 10) + k;
    return s >= 9 && (k == 4 || k == 7) ? 200 + 2 * (s - 9) + (k == 7) : -1;
  }
  if (s == 0 || s == 6) return k % 3 == 0 ? 100 + 3 * (s / 6 + 1) + k / 3 : -1;
  if (s == 1) return -1;
  if (s <= 8) return (k & 1) ? -1 : QPRE1 + 4 * (s - 2 - (s > 6)) + k / 2;
  return k >= 3 ? 300 + 5 * (s - 9) + k - 3 : -1;
}
constexpr int qop_size(int o) { return o < 0 ? 0 : o >= 100 && o < 200 ? ((o - 100) % 3 == 2 ? 2 : 3) : 1; }
constexpr int qop_younger(bool l0, int o, int s) {
  const bool pre = l0 ? o < QPRE0 : o < QPRE1 || o == 102;
  int n = !pre ? 0 : l0 ? QPRE0 - 1 - o : o < QPRE1 ? 8 : 0;
  bool seen = pre;
  for (int t = 0; t < s; ++t)
    for (int k = 0; k < 8; ++k) { if (seen) n += qop_size(qop(l0, t, k)); if (qop(l0, t, k) == o) seen = true; }
  return seen ? (n < 63 ? n : 63) : -1;
}
constexpr int imin(int a, int b) { return a < b ? a : b; }
constexpr int qop_wait(bool l0, int s) {
  return s % 6 == 0 && (s || !l0) ? imin(qop_younger(l0, 4 * s + 3, s), qop_younger(l0, 100 + s / 2 + 2, s)) : qop_younger(l0, 4 * s + 3, s);
}

__global__ __launch_bounds__(256, 1) void k_main(const u16* __restrict__ x, const u16* __restrict__ wqkv, const u16* __restrict__ wo,
                                                 const u16* __restrict__ wg, const u16* __restrict__ wu, const u16* __restrict__ wd,
                                                 unsigned char* __restrict__ ws, u16* __restrict__ out) {
  extern __shared__ __attribute__((aligned(16))) unsigned char smem_[];
  LAS unsigned char* L = (LAS unsigned char*)smem_;
  const unsigned Lb = (unsigned)(uintptr_t)L;
  int w = __builtin_amdgcn_readfirstlane(threadIdx.x >> 6);
  int j = (blockIdx.x >> 3) & 7, g = 4 * (blockIdx.x & 7) + (blockIdx.x >> 6);
  int jc = blockIdx.x & 7, gc = blockIdx.x >> 3;
#define FRESH() asm volatile("" : "+s"(w), "+s"(j), "+s"(g), "+s"(jc), "+s"(gc), "+s"(l))
  const rsrc_t rs = __builtin_amdgcn_make_buffer_rsrc(ws, 0, WS_TOTAL, 0x00020000);
  u64 tag, pc0;
  int code_len;
  asm volatile("s_getpc_b64 %0\n.Lk_main_pc:\n\ts_mov_b32 %1, .Lk_main_end-.Lk_main_pc" : "=s"(pc0), "=s"(code_len));
  {
    const int l = 0, lane = threadIdx.x & 63, r = lane & 15, q = lane >> 4, p = lane & 3;
#define DMAV(m0v, voff, rsv, so) asm volatile("s_mov_b32 m0, %0\n\ts_nop 0\n\tbuffer_load_dwordx4 %1, %2, %3 offen nt lds" :: "s"(m0v), "v"(voff), "s"(rsv), "s"(so) : "memory")
    if (w == 3) {
      code_touch(pc0, code_len, gc, lane);
      touch(rs, lane < 10 ? fg_off(lane, gc) + jc * SS : lane < 12 ? OFF_FQ + ((lane - 10) * 256 + blockIdx.x) * SS : lane == 12 ? OFF_FQ + (512 + (blockIdx.x & 63)) * SS : OOB, 0);
    }
    const int n = (gc - 27) * 8 + jc;
    const bool isq = gc < 18, iso = gc >= 27 && n < 36;
    v4u xr[3];
#pragma unroll
    for (int i = 0; i < 3; ++i)
      asm volatile("global_load_dwordx4 %0, %1, %2 offset:%3" : "=v"(xr[i]) : "v"(((64 * g + 8 * j + (lane & 7)) * D_ + (24 * (w < 3 ? w : 0) + (lane >> 3)) * 8) * 2), "s"(x), "i"(i * 128) : "memory");
    if (isq) {
      const rsrc_t rq = __builtin_amdgcn_make_buffer_rsrc((void*)wqkv, 0, D_ * 1728 * 2, 0x00020000);
      const int c = lane & 31, voff = c < 27 ? (lane >> 5) * 3456 + (c < 9 ? 1152 : c < 18 ? 2304 - 144 : -288) + c * 16 : OOB;
#pragma unroll
      for (int k = 0; k < 4; ++k) DMAV(Lb + (4 * w + k) * 1024, voff, rq, (32 * gc + 8 * w + 2 * k) * 3456 + jc * 144);
    } else if (iso) {
      const rsrc_t ro = __builtin_amdgcn_make_buffer_rsrc((void*)wo, 0, D_ * D_ * 2, 0x00020000);
      const int pg = 4 * (n >> 1) + w, hg = pg / 9, ii = pg - 9 * hg, voff = lane < 36 ? lane * 16 : OOB;
#pragma unroll
      for (int jj = 0; jj < 8; ++jj) {
        const int R = ii < 8 ? (4 * hg + (ii >> 1)) * 18 + (ii & 2) + (ii & 1) * 8 + jj : (4 * hg + (jj >> 1)) * 18 + ((jj & 2) ? 0 : 16) + (jj & 1);
        DMAV(Lb + (8 * w + jj) * 1024, voff, ro, R * (D_ * 2) + (n & 1) * 576);
      }
    }
    asm volatile("s_load_dwordx2 %0, %1, 0x0\n\ts_waitcnt lgkmcnt(0)" : "=s"(tag) : "s"(ws) : "memory");
    tag += 1;
    asm volatile("s_waitcnt vmcnt(0)" : "+v"(xr[0]), "+v"(xr[1]), "+v"(xr[2]) :: "memory");
    if (w == 3) {
      BAR();
    } else {
      float sum = 0.f, sq = 0.f;
#pragma unroll
      for (int i = 0; i < 3; ++i)
#pragma unroll
        for (int e = 0; e < 4; ++e) { sum = dot2(xr[i][e], 0x3f803f80u, sum); sq = dot2(xr[i][e], xr[i][e], sq); }
      const int v0 = (lane >> 3) * 256 + (lane & 7) * 16, vo[3] = {v0, v0 + 2048, v0 + 4096};
      ln_rows(L, rs, xr, sum, sq, OFF_XN + (4 * g + (j >> 1)) * TT_BYTES + (j & 1) * 128 + w * 6144, vo, w, lane);
    }
    if (isq) {
      LAS unsigned char* tp = L + (8 * q + (r >> 2)) * 512 + 36 * w - 4 * (w & 1);
#pragma unroll
      for (int a = 0; a < 4; ++a) {
        const int c = a == 2 ? (p < 3 ? p : 0) * 144 + ((w & 1) ? 0 : 32) : (a == 3 ? 2 : a) * 144 + 8 * (w & 1) + 8 * p;
        bstore(tr_frag(tp + c, tp + c + 2048), rs, lane * 16, OFF_WQ + ((4 * jc + w) * 72 + 4 * gc + a) * 1024);
      }
    } else if (iso) {
      LAS unsigned char* tp = L + (8 * q + (r >> 2)) * 1024 + 8 * p;
#pragma unroll 1
      for (int T = 3 - w; T < 18; T += 4) {
        const int T3 = T / 3;
        bstore(tr_frag(tp + 32 * T, tp + 32 * T + 4096), rs, lane * 16, OFF_WO + ((6 * (n & 1) + T3) * 54 + 3 * (n >> 1) + T - 3 * T3) * 1024);
      }
    }
    DRAIN();
    BAR();
    if (w == 3) {
      publish(rs, fg_off(7, g) + j * SS, tag, lane);
      if (isq || iso) publish(rs, isq ? OFF_FQ + jc * 4096 + gc * SS : OFF_FQ + (8 + (n & 1)) * 4096 + (n >> 1) * SS, tag, lane);
      wait_slots(rs, lane >= 48 ? (lane < 56 || g == 0 ? fg_off(7, g) + (lane & 7) * SS : fg_off(7, g - 1) + (6 + (lane & 1)) * SS)
                                : lane < 18 ? OFF_FQ + j * 4096 + lane * SS : OFF_FQ + (8 + ((j >> 1) & 1)) * 4096 + (lane < 36 ? lane - 18 : 0) * SS, tag, lane);
    } else if (gc < 27) {
      const int type = gc / 9, cb = type == 2 ? gc - 18 : 9 * jc + gc - 9 * type, pitch = type < 2 ? F_ * 2 : D_ * 2, jj = lane >> 3;
      const rsrc_t rf = __builtin_amdgcn_make_buffer_rsrc((void*)(type == 0 ? wg : type == 1 ? wu : wd), 0, D_ * F_ * 2, 0x00020000);
      const int fv = jj * pitch + (((lane & 7) ^ (jj & 2)) << 4), fs = cb * 128 + ((type < 2 ? 0 : 576 * jc) + 192 * w) * pitch;
#pragma unroll 2
      for (int k = 0; k < 24; ++k) DMAV(Lb + TW_OFF + (24 * w + k) * 1024, fv ^ ((k & 1) << 6), rf, fs + k * 8 * pitch);
    }
    BAR();
  }
  asm volatile("" ::: "a0", "a255");

#pragma unroll 1
  for (int l = 0; l < 2; ++l) {
    int tid = threadIdx.x;
    asm volatile("" : "+v"(tid));
    const int lane = tid & 63, tok = lane & 15, q = lane >> 4, lane16 = lane * 16;
    __builtin_amdgcn_s_waitcnt(0xC07F);
    FRESH();
    if (l != 0) {
      const int wq_b = OFF_WQ + (4 * j + w) * 72 * 1024;
      if (w == 3) {
        publish(rs, fg_off(2, gc) + jc * SS, tag, lane);
        sfor<0, DW>([&](auto i) { wload<i, i>(rs, lane16, wq_b); });
      } else {
        phase_d<false>(L, rs, out, tag, 0, g, j, w, lane);
      }
      sfor<DW, 18>([&](auto i) { wload<i, i>(rs, lane16, wq_b); });
      sfor<36, QPRE1>([&](auto i) { wload<i, i>(rs, lane16, wq_b); });
      if (w == 3) {
        wait_slots<true>(rs, fg_off(2, g) + (lane & 7) * SS, tag, lane);
        BAR();
        BAR();
        BAR();
        publish(rs, fg_off(3, g) + j * SS, tag, lane);
        wait_slots(rs, lane < 8 || g == 0 ? fg_off(3, g) + (lane & 7) * SS : fg_off(3, g - 1) + (6 + (lane & 1)) * SS, tag, lane);
      }
      BAR();
    }
    FRESH();
    const int rv = ((q >> 1) * 16 + tok) * 16 + (q & 1) * TT_BYTES;
    int wb = OFF_WF + (4 * jc + w) * (RUN_FR * 1024);
    {
      const int xoff = OFF_XN + l * XF_BYTES;
      const int tth = g == 0 ? 0 : 4 * g - 1;
      const unsigned m0 = Lb + (w + 1) * 6144, so = xoff + (4 * g + w) * TT_BYTES;
      const int hw = w < 3 ? w : 2;
      const unsigned m0h = Lb + hw * 2048, soh = xoff + tth * TT_BYTES + hw * 2048;
      auto xpiece = [&](auto c, auto i) __attribute__((always_inline)) {
        if constexpr (i < 2) dma<3>(rs, m0 + (c & 1) * 30720 + i * 3072, lane16, so + c * 6144 + i * 3072);
        else dma<2>(rs, m0h + (c & 1) * 30720, lane16, soh + c * 6144);
      };
      const int wq2 = OFF_WQ + (4 * j + w) * 72 * 1024;
      f32x4 acc[4][5];
      LAS unsigned char* lp0 = L + lane16;
#define XA(t, s) (*(LAS v4u*)(lp0 + (((s) / 6) & 1) * 30720 + (t) * 6144 + ((s) % 6) * 1024))
      int fb0 = OOB, fst = 0, fb1 = OOB, fst1 = 0, fcs = OOB, fr16 = 0, frh = 0;
      LAS unsigned char* ftp = L;
      if (l == 0 && gc < 27) {
        const int type = gc / 9, T = 4 * (type == 2 ? gc - 18 : 9 * jc + gc - 9 * type) + w, run = T / 9, i = T - 9 * run, rr = tok >> 2, p = lane & 3;
        if (type < 2) {
          const int cbyte = 32 * w + 8 * p;
          fb0 = OFF_WF + (run * RUN_FR + 2 * i + type) * 1024; fst = 18 * 1024;
          fb1 = OFF_WF + (run * RUN_FR + 288 + 4 * i + type) * 1024; fst1 = 2048;
          fcs = OFF_CSG + (run * 18 + 2 * i + type) * 64;
          ftp = L + TW_OFF + (8 * q + rr) * 128 + ((((cbyte >> 4) ^ (2 * (rr >> 1) + 4 * (q & 1))) << 4) + (cbyte & 8));
        } else {
          const int a = (run & 1) ? (i == 0 ? 8 : i - 1) : i, cbyte = 64 * (w >> 1) + 16 * p + 8 * (w & 1);
          fb0 = OFF_WF + ((4 * jc + run) * RUN_FR + 324 + (a < 8 ? (a >> 1) * 36 + (a & 1) : 144)) * 1024; fst = a < 8 ? 2048 : 1024;
          fb1 = fb0 + 16 * fst; fst1 = fst;
          fr16 = 16 * 128; frh = 12 * 128;
          ftp = L + TW_OFF + (4 * q + rr) * 128 + ((((cbyte >> 4) ^ (2 * (rr >> 1) + 4 * (q >> 1))) << 4) + (cbyte & 8));
        }
      }
      float fsum = 0.f;
#define IC(x) std::integral_constant<int, (x)>{}
      const int nx1 = w < 3 ? OFF_WO + ((3 * (j & 3) + w) * 54 - 10) * 1024 : OOB;
      auto qkv = [&](auto l0) __attribute__((always_inline)) {
        v4u B[2][5];
        sfor<0, 3>([&](auto i) { xpiece(IC(0), i); });
        if constexpr (l0) sfor<0, QPRE0>([&](auto i) { wload<i, i>(rs, lane16, wq2); });
        sfor<0, KS>([&](auto s) {
          if constexpr (l0 || s % 6 == 0 || s >= 12) wait_vm<qop_wait(l0, s)>();
          if constexpr (s % 6 == 0) {
            BAR();
#pragma unroll
            for (int t = 0; t < 5; ++t) B[s & 1][t] = XA(t, s);
            SB();
          }
          sfor<0, 4>([&](auto a) {
            sfor<0, 5>([&](auto t) {
              if constexpr (!(a == 3 && t == 0)) mf_av<(4 * s + a) % 64, s == 0>(acc[a][t], B[s & 1][t]);
              if constexpr (a == 0 && (s + 1) % 6 != 0) { B[(s + 1) & 1][t] = XA(t, s + 1); SB(); }
              if constexpr (t == 1 || t == 3) {
                constexpr int o = qop(l0, s, 2 * a + t / 2);
                if constexpr (o >= 300) wload<o - 290, o - 290>(rs, lane16, nx1);
                else if constexpr (o >= 200) {
                  constexpr int f = o - 200;
                  LAS unsigned char* p0 = ftp + f * 4096 + (f < 16 ? f / 4 : f == 16 ? -24 : -8) * fr16;
                  const v4u fo = tr_frag(p0, p0 + 512 + frh + (f < 16 ? 0 : 8) * fr16);
                  bstore(fo, rs, lane16, f < 16 ? fb0 + f * fst : fb1 + (f - 16) * fst1);
#pragma unroll
                  for (int e = 0; e < 4; ++e) fsum = dot2(fo[e], 0x3f803f80u, fsum);
                  SB();
                }
                else if constexpr (o >= 100) xpiece(IC((o - 100) / 3), IC((o - 100) % 3));
                else if constexpr (o >= 0) wload<o % 64, o>(rs, lane16, wq2);
              }
            });
          });
        });
        if constexpr (l0) {
          fsum = sum_q(fsum);
          if (lane < 16) __builtin_amdgcn_raw_buffer_store_b32(__builtin_bit_cast(unsigned, fsum), rs, lane * 4, fcs, SC1);
        }
        asm volatile("s_nop 7\n\ts_barrier" ::: "memory");
      };
      if (l == 0) qkv(std::true_type{}); else qkv(std::false_type{});
      SB();
      FRESH();
      touch(rs, l != 0 && lane < 36 ? (gc * 36 + lane) * 128 : OOB, wb);
      LAS unsigned char* E = L + E_OFF + w * (80 * EROW);
      auto ewrite = [&](int a) __attribute__((always_inline)) {
#pragma unroll
        for (int t = (a == 3); t < 5; ++t) {
          const f32x4 v = acc[a][t];
          if (a == 2) *(LAS unsigned*)(E + (16 * t + tok) * EROW + 64 + q * 4) = pack2((w & 1) ? v[2] : v[0], (w & 1) ? v[3] : v[1]);
          else *(LAS v2u*)(E + (16 * t + tok) * EROW + (a == 3 ? 80 : a * 32) + q * 8) = v2u{pack2(v[0], v[1]), pack2(v[2], v[3])};
        }
        asm volatile("" ::: "memory");
        SB();
      };
      ewrite(0); ewrite(3);
      LAS unsigned char* er = E + (2 + lane) * EROW;
      const v4u q0 = *(LAS v4u*)(er + 14 * EROW + 80);
      v4u k0[8], k1[8], v0[8], v1[8];
      v2u kv[8];
#pragma unroll
      for (int i = 0; i < 8; ++i) k0[i] = *(LAS v4u*)(er + (14 - 2 * i) * EROW);
      SB();
      ewrite(2); ewrite(1);
      const v4u q1 = *(LAS v4u*)(er + 14 * EROW + 96);
#pragma unroll
      for (int i = 0; i < 4; ++i) k1[i] = *(LAS v4u*)(er + (14 - 2 * i) * EROW + 16);
      SB();
      float lg[8];
#pragma unroll
      for (int i = 0; i < 8; ++i) {
        lg[i] = dot2(q0[3], k0[i][3], dot2(q0[2], k0[i][2], dot2(q0[1], k0[i][1], dot2(q0[0], k0[i][0], 0.f))));
        if (i < 4) k1[4 + i] = *(LAS v4u*)(er + (6 - 2 * i) * EROW + 16);
        else { kv[2 * i - 8] = *(LAS v2u*)(er + (30 - 4 * i) * EROW + 64); SB(); kv[2 * i - 7] = *(LAS v2u*)(er + (28 - 4 * i) * EROW + 64); }
        SB();
      }
      const unsigned q2 = *(LAS unsigned*)(er + 14 * EROW + 72);
#pragma unroll
      for (int i = 0; i < 8; ++i) {
        lg[i] = dot2(q1[3], k1[i][3], dot2(q1[2], k1[i][2], dot2(q1[1], k1[i][1], dot2(q1[0], k1[i][0], lg[i]))));
        v0[i] = *(LAS v4u*)(er + (14 - 2 * i) * EROW + 32);
        SB();
      }
#pragma unroll
      for (int i = 0; i < 8; ++i) {
        lg[i] = dot2(q2, kv[i][0], lg[i]);
        v1[i] = *(LAS v4u*)(er + (14 - 2 * i) * EROW + 48);
      }
      SB();
      if (g == 0) {
#pragma unroll
        for (int i = 1; i < 8; ++i) lg[i] = lane >= 2 * i ? lg[i] : -3e38f;
      }
      const float sc = 0.23570226039551584f * 1.4426950408889634f;
      const float mx = max3(max3(lg[0], lg[1], lg[2]), max3(lg[3], lg[4], lg[5]), max3(lg[6], lg[7], lg[7]));
      const float nb_ = -mx * sc;
      unsigned pp[4];
      float sum = 0.f;
#pragma unroll
      for (int i = 0; i < 4; ++i) {
        pp[i] = pack2(__builtin_amdgcn_exp2f(fmaf(lg[2 * i], sc, nb_)), __builtin_amdgcn_exp2f(fmaf(lg[2 * i + 1], sc, nb_)));
        sum = dot2(pp[i], 0x3f803f80u, sum);
      }
      const float inv = __builtin_amdgcn_rcpf(sum);
      const int ob = OFF_O + 4 * g * TT_BYTES + 9 * j * 256, ov = q * TT_BYTES + tok * 16;
      auto pv = [&](unsigned a0, unsigned a1, unsigned a2, unsigned a3, unsigned a4, unsigned a5, unsigned a6, unsigned a7) __attribute__((always_inline)) {
        const unsigned a[8] = {a0, a1, a2, a3, a4, a5, a6, a7};
        float lo = 0.f, hi = 0.f;
#pragma unroll
        for (int i = 0; i < 4; ++i) {
          lo = dot2(__builtin_amdgcn_perm(a[2 * i + 1], a[2 * i], 0x05040100u), pp[i], lo);
          hi = dot2(__builtin_amdgcn_perm(a[2 * i + 1], a[2 * i], 0x07060302u), pp[i], hi);
        }
        return pack2(lo * inv, hi * inv);
      };
#define PV(v, e) pv(v[0][e], v[1][e], v[2][e], v[3][e], v[4][e], v[5][e], v[6][e], v[7][e])
      __builtin_amdgcn_raw_buffer_store_b32(PV(kv, 1), rs, ov, ob + 8 * 256 + 4 * w, SC1);
      SB();
      bstore(v4u{PV(v0, 0), PV(v0, 1), PV(v0, 2), PV(v0, 3)}, rs, ov, ob + 2 * w * 256);
      SB();
      bstore(v4u{PV(v1, 0), PV(v1, 1), PV(v1, 2), PV(v1, 3)}, rs, ov + 256, ob + 2 * w * 256);
      DRAIN();
      BAR();
    }
    FRESH();
    if (w == 3) {
      publish(rs, fg_off(4 * l, g) + j * SS, tag, lane);
      if (l == 0) publish(rs, OFF_FF + jc * 4096 + gc * SS, tag, lane);
      wait_slots(rs, fg_off(4 * l, g) + (lane & 7) * SS, tag, lane);
      BAR();
      code_touch(pc0, code_len, gc, lane);
      BAR();
      if (l == 0) wait_slots<true>(rs, OFF_FF + jc * 4096 + (lane < 27 ? lane : 0) * SS, tag, lane);
      sfor<0, 36>([&](auto i) { wload<i, i>(rs, lane16, wb); });
      if (l == 0) {
        dma<3>(rs, Lb + CS_OFF, lane16, OFF_CSG + jc * 4608);
        dma<2>(rs, Lb + CS_OFF + 3072, lane16, OFF_CSG + jc * 4608 + 3072);
        tail_dma(rs, Lb, jc, lane16, 0, 24);
      }
      BAR();
      publish(rs, fg_off(4 * l + 1, g) + j * SS, tag, lane);
      wait_slots(rs, fg_off(4 * l + 1, gc) + (lane & 7) * SS, tag, lane);
      BAR();
    } else {
      const int nb = j & 3, tt0 = 4 * g + 2 * (j >> 2);
      v4u res[3];
      {
        const rsrc_t rx = __builtin_amdgcn_make_buffer_rsrc(l == 0 ? (void*)x : (void*)(ws + OFF_X), 0, XF_BYTES, 0x00020000);
        const int as = l == 0 ? 32 : 512, xv_ = l == 0 ? tok * (D_ * 2) + (q >> 1) * 16 + (q & 1) * TT_BYTES : rv, rb = tt0 * TT_BYTES + (9 * nb + 3 * w) * as;
#pragma unroll
        for (int a = 0; a < 3; ++a) res[a] = bload<SC1>(rx, xv_, rb + a * as);
      }
      if (l == 0) sfor<10 + QWB0, 10 + QWB1>([&](auto i) { wload<i, i>(rs, lane16, OFF_WO + ((3 * nb + w) * 54 - 10) * 1024); });
      sfor<10 + QWB1, 64>([&](auto i) { wload<i, i>(rs, lane16, OFF_WO + ((3 * nb + w) * 54 - 10) * 1024); });
      const unsigned om0 = Lb + w * 12288, oso = OFF_O + tt0 * TT_BYTES + w * 12288;
      BAR();
#pragma unroll
      for (int c = 0; c < 4; ++c) dma<3>(rs, om0 + c * 3072, lane16, oso + c * 3072);
      DRAIN();
      BAR();
      LAS unsigned char* lp = L + lane16;
      v4u Bo[KS][2];
#pragma unroll
      for (int s = 0; s < 3; ++s) { Bo[s][0] = *(LAS v4u*)(lp + s * 1024); Bo[s][1] = *(LAS v4u*)(lp + TT_BYTES + s * 1024); }
      SB();
      f32x4 acc[3][2];
      const int wb1 = l == 0 ? OOB : wb;
      sfor<0, KS>([&](auto s) {
        sfor<0, 3>([&](auto a) {
          mf_av<10 + 3 * s + a, s == 0>(acc[a][0], Bo[s][0]);
          if constexpr (a < 2 && s + 3 < KS) { Bo[s + 3][a] = *(LAS v4u*)(lp + a * TT_BYTES + (s + 3) * 1024); SB(); }
          mf_av<10 + 3 * s + a, s == 0>(acc[a][1], Bo[s][1]);
          if constexpr (a == 2 && s < RB) wload<s, s>(rs, lane16, wb1);
        });
      });
      SB();
      asm volatile("s_nop 7");
      SB();
      const int rb = tt0 * TT_BYTES + (9 * nb + 3 * w) * 512;
      float sum = 0.f, sq = 0.f;
#pragma unroll
      for (int a = 0; a < 3; ++a) {
        float y[8];
#pragma unroll
        for (int e = 0; e < 4; ++e) {
          const auto sw = __builtin_amdgcn_permlane16_swap(__builtin_bit_cast(unsigned, (float)acc[a][0][e]), __builtin_bit_cast(unsigned, (float)acc[a][1][e]), false, false);
          y[e] = asf(sw[0]); y[4 + e] = asf(sw[1]);
        }
        v4u o;
#pragma unroll
        for (int e = 0; e < 4; ++e) {
          o[e] = pack2(bflo(res[a][e]) + y[2 * e], bfhi(res[a][e]) + y[2 * e + 1]);
          sum = dot2(o[e], 0x3f803f80u, sum);
          sq = dot2(o[e], o[e], sq);
        }
        bstore(o, rs, rv + a * 512, OFF_X1 + rb);
      }
      const auto s1 = __builtin_amdgcn_permlane32_swap(__builtin_bit_cast(unsigned, sum), __builtin_bit_cast(unsigned, sq), false, false);
      __builtin_amdgcn_raw_buffer_store_b32(__builtin_bit_cast(unsigned, asf(s1[0]) + asf(s1[1])), rs, tok * 16 + (q & 1) * 8 + (q >> 1) * 4,
                                            OFF_SC + ((tt0 >> 1) * 12 + 3 * nb + w) * 256, SC1);
      DRAIN();
      BAR();
      if (l == 0) sfor<0, RB>([&](auto i) { wload<i, i>(rs, lane16, wb); });
      sfor<RB, 36>([&](auto i) { wload<i, i>(rs, lane16, wb); });
    }
    FRESH();
    if (w != 3) BAR();
    {
      FRESH();
      unsigned xm0 = Lb + w * XS;
      unsigned xso = OFF_X1 + (4 * gc + w) * TT_BYTES;
      f32x4 pm[6];
#pragma unroll
      for (int i = 0; i < 6; ++i)
        asm volatile("buffer_load_dwordx4 %0, %1, %2, %3 offen offset:%4 sc1" : "=v"(pm[i]) : "v"(tok * 16 + q * 768), "s"(rs), "s"(OFF_SC + gc * 6144), "i"((i / 3) * 3072 + (i % 3) * 256) : "memory");
      dma<3>(rs, xm0, lane16, xso);
      dma<3>(rs, xm0 + 3072, lane16, xso + 3072);
      f32x4 cs[18];
#pragma unroll
      for (int a = 0; a < 18; ++a) cs[a] = *(LAS f32x4*)(L + CS_OFF + w * 1152 + q * 16 + a * 64);
      SB();
      asm volatile("s_waitcnt vmcnt(6)" : "+v"(pm[0]), "+v"(pm[1]), "+v"(pm[2]), "+v"(pm[3]), "+v"(pm[4]), "+v"(pm[5]) :: "memory");
      f32x4 acc[18][4];
      {
        float nm[4];
#pragma unroll
        for (int t = 0; t < 4; ++t) nm[t] = rbf(sum_q(pm[3 * (t >> 1)][2 * (t & 1)] + pm[3 * (t >> 1) + 1][2 * (t & 1)] + pm[3 * (t >> 1) + 2][2 * (t & 1)]) * (-1.0f / D_));
        sfor<0, 18>([&](auto ai) {
          constexpr int a = (ai + 2) % 18;
          sfor<0, 4>([&](auto t) {
            if constexpr (acc_in_a(a)) quad_write<accq(a, t)>(cs[a] * nm[t]);
            else { acc[a][t] = cs[a] * nm[t]; asm volatile("" : "+v"(acc[a][t])); }
          });
          SB();
        });
      }
      DRAIN();
      BAR();
      LAS unsigned char* lp = L + lane16;
      sfor<0, 4>([&](auto t) { lload<BQ + t, t * XS>(lp); });
      wait_lgkm<0>();
      wb += 36 * 1024;
      xm0 += 6144; xso += 6144;
      auto g1mf = [&](auto a, auto t, auto qa, auto qb) __attribute__((always_inline)) {
        if constexpr (acc_in_a(a)) mf_aaa<accq(a, t), qa, qb>(); else mf_aa<qa, qb>(acc[a][t]);
      };
#pragma unroll 1
      for (int it = 0; it < 8; ++it) {
        sfor<0, 36>([&](auto i) {
          constexpr int a = i % 18, sec = i / 18;
          wait_vm<i == 0 ? 35 : 34>();
          sfor<0, 4>([&](auto t) {
            g1mf(IC(a), t, i, IC(BQ + 4 * sec + t));
            if constexpr (t == 0 && i > 0) wload<i - 1, i - 1>(rs, lane16, wb);
            if constexpr (t == 1 && a >= 2 && a < 6) lload<BQ + 4 * (1 - sec) + a - 2, (a - 2) * XS + 1024 * (1 + sec)>(lp);
            if constexpr (t == 1 && a == 12) wait_lgkm<0>();
            if constexpr (t == 1 && i == 18) BAR();
            if constexpr (t == 1 && i == 27) dma2_if_lt6(it, rs, xm0, lane16, xso);
          });
        });
        wload<35, 35>(rs, lane16, wb);
        lp += 2048;
        wb += 36 * 1024;
        xm0 += 2048; xso += 2048;
      }
      {
        unsigned hreg[18][4];
        LAS unsigned char* hp = L + lane16 + 4 * w * 1024;
        sfor<0, 36>([&](auto gi) {
          constexpr int a = (gi / 4) * 2 + (gi & 1), sec = (gi >> 1) & 1;
          constexpr int ep = gi / 4 - 1, et = gi & 3;
          wait_vm<gi == 0 ? 35 : 34>();
          if constexpr (gi == 2) wait_lgkm<0>();
          if constexpr (gi == 3) touch(rs, lane < 27 ? (gc * 27 + lane) * 128 : OOB, wb + 36864);
          if constexpr (ep < 0) {
            sfor<0, 4>([&](auto t) {
              g1mf(IC(a), t, gi, IC(BQ + 4 * sec + t));
              if constexpr (gi == 0) lload<BQ + 4 + t, t * XS + 1024>(lp);
              if constexpr (t == 0 && gi > 0) wload<gi - 1, gi - 1>(rs, lane16, wb);
            });
          } else {
            constexpr int ev = acc_in_a(2 * ep) ? 16 : 2 * ep;
            group_h<gi, BQ + 4 * sec, acc_in_a(a) ? accq(a, 0) : -1, acc_in_a(2 * ep) ? accq(2 * ep, et) * 4 : -1>(
                acc[acc_in_a(a) ? 16 : a], acc[ev][et], acc[ev + 1][et], hreg[2 * ep][et], hreg[2 * ep + 1][et], rs, lane16, wb);
            if constexpr (ep & 1) {
              *(LAS v4u*)(hp + et * XS + (ep >> 1) * 1024) = v4u{hreg[2 * ep - 2][et], hreg[2 * ep - 1][et], hreg[2 * ep][et], hreg[2 * ep + 1][et]};
              SB();
            }
          }
        });
        wload<35, 35>(rs, lane16, wb);
        LAS unsigned char* hq = L + lane16 + (18 + (w >> 1)) * 1024 + (w & 1) * 8;
#pragma unroll
        for (int t = 0; t < 4; ++t) {
          const unsigned h0 = hpair(acc[16][t][0], acc[16][t][1], acc[17][t][0], acc[17][t][1]);
          const unsigned h1 = hpair(acc[16][t][2], acc[16][t][3], acc[17][t][2], acc[17][t][3]);
          *(LAS v2u*)(hq + t * XS) = v2u{h0, h1};
          SB();
        }
      }
      asm volatile("s_waitcnt lgkmcnt(0)\n\ts_barrier" ::: "memory");
      wb += 36 * 1024;
      LAS unsigned char* hl0 = L + lane16 + (w & 1) * 2 * XS;
      LAS unsigned char* hl1 = L + lane16 + (1 - (w & 1)) * 2 * XS;
      asm volatile("" : "+v"(hl1));
      constexpr int HV = 11;
#define HOFF(s, t) (((t) & 1) * XS + ((s) < 16 ? (s) : (s) + 2) * 1024)
#define HPTR(t) ((t) < 2 ? hl0 : hl1)
      v4u Hf[HV][4];
#pragma unroll
      for (int s = 0; s < 2; ++s)
#pragma unroll
        for (int t = 0; t < 4; ++t) Hf[s][t] = *(LAS v4u*)(HPTR(t) + HOFF(s, t));
      SB();
      FRESH();
      const int s0 = (9 * w + (w & 1)) >> 1;
      int pso = OFF_P + jc * XF_BYTES + (4 * gc + 2 * (w & 1)) * TT_BYTES;
      const int ph = (w & 1) ? -2 * TT_BYTES : 2 * TT_BYTES;
      const int pso8 = pso + (4 + 9 * (w >> 1)) * 1024;
      pso += s0 * 1024;
#define PT(t) (((t) & 1) * TT_BYTES + ((t) >> 1) * ph)
      f32x4 c0[4], c1[4];
      v4u pk[4];
      auto pair_iter = [&](auto first, int na_, int nb_) {
        sfor<0, 36>([&](auto i) {
          constexpr int s = i / 2;
          constexpr int nst = first ? 0 : (i <= 4 ? 0 : i <= 10 ? 1 : i <= 16 ? 2 : i <= 22 ? 3 : 4);
          wait_vm<(i == 0 ? 35 : 34) + nst>();
          if constexpr (first && s >= HV && (i & 1) == 0) wait_lgkm<(s + 1 < KS ? 4 : 0)>();
          sfor<0, 4>([&](auto t) {
            f32x4& c = (i & 1) ? c1[t] : c0[t];
            if constexpr (!first && s == 0) { pk[t][2 * i] = pack2(c[0], c[1]); pk[t][2 * i + 1] = pack2(c[2], c[3]); SB(); }
            if constexpr (s < HV) mf_av<i, s == 0>(c, Hf[s][t]); else mf_aa<i, 36 + (s - HV) * 4 + t>(c);
            if constexpr (t == 0 && i > 0) wload<i - 1, i - 1>(rs, lane16, i - 1 < 18 ? na_ : nb_);
            if constexpr (first && s + 2 < KS && t >= 1 && t < 3) {
              constexpr int ht = (i & 1) * 2 + t - 1;
              if constexpr (s + 2 < HV) { Hf[s + 2][ht] = *(LAS v4u*)(HPTR(ht) + HOFF(s + 2, ht)); SB(); }
              else lload<36 + (s + 2 - HV) * 4 + ht, HOFF(s + 2, ht)>(HPTR(ht));
            }
            if constexpr (!first && t == 2 && i >= 4 && i <= 22 && (i - 4) % 6 == 0) {
              __builtin_amdgcn_raw_buffer_store_b128(pk[(i - 4) / 6], rs, lane16, pso + PT((i - 4) / 6), SC1);
              SB();
            }
          });
        });
        wload<35, 35>(rs, lane16, nb_);
        wb += 36 * 1024;
        if constexpr (!first) pso += 1024;
      };
      pair_iter(std::true_type{}, wb, wb);
      const int wnext = l == 0 ? OFF_WQ + (4 * j + w) * 72 * 1024 : OOB;
#pragma unroll 1
      for (int p = 1; p < 4; ++p) { pair_iter(std::false_type{}, p < 3 ? wb : OOB, p < 3 ? wb : wnext); }
      {
        LAS unsigned char* tw = L + TW_OFF + (w >> 1) * 2 * TT_BYTES + lane16;
        v4u At[KS][2];
#pragma unroll
        for (int s = 0; s < 2; ++s) { At[s][0] = *(LAS v4u*)(tw + s * 1024); At[s][1] = *(LAS v4u*)(tw + TT_BYTES + s * 1024); }
        SB();
#pragma unroll
        for (int t = 0; t < 4; ++t) { pk[t][0] = pack2(c0[t][0], c0[t][1]); pk[t][1] = pack2(c0[t][2], c0[t][3]); }
        SB();
        sfor<0, KS>([&](auto s) {
          sfor<0, 4>([&](auto t) {
            if constexpr (s < HV) mf_vv<s == 0>(c0[t], At[s][t / 2], Hf[s][t & 1]); else mf_va<36 + (s - HV) * 4 + (t & 1)>(c0[t], At[s][t / 2]);
            if constexpr (s == 0) { pk[t][2] = pack2(c1[t][0], c1[t][1]); pk[t][3] = pack2(c1[t][2], c1[t][3]); SB(); }
            if constexpr ((t & 1) && s + 2 < KS) { At[s + 2][t / 2] = *(LAS v4u*)(tw + (t / 2) * TT_BYTES + (s + 2) * 1024); SB(); }
            if constexpr (t == 3 && s == 0) {
              wait_vm<14>();
              BAR();
              if (w == 3) publish(rs, fg_off(8 + l, gc) + jc * SS, tag, lane);
            }
            if constexpr (t == 2 && s >= 1 && s <= 4) { __builtin_amdgcn_raw_buffer_store_b128(pk[s - 1], rs, lane16, pso + PT(s - 1), SC1); SB(); }
            if constexpr (t == 2 && s == 17) peek_slots(rs, (lane & 7) * SS + fg_off(8 + l, g));
          });
        });
        asm volatile("s_nop 7");
        SB();
#pragma unroll
        for (int t = 0; t < 2; ++t)
          bstore(v4u{pack2(c0[t][0], c0[t][1]), pack2(c0[t][2], c0[t][3]), pack2(c0[2 + t][0], c0[2 + t][1]), pack2(c0[2 + t][2], c0[2 + t][3])}, rs, lane16, pso8 + t * TT_BYTES);
      }
      DRAIN();
      BAR();
    }
  }
  {
    const int l = 1, lane = threadIdx.x & 63;
    if (w == 3) {
      publish(rs, fg_off(6, gc) + jc * SS, tag, lane);
      wait_slots(rs, fg_off(6, g) + (lane & 7) * SS, tag, lane);
      BAR();
      if (blockIdx.x == 0) {
        wait_all_started(rs, tag, lane);
        publish(rs, OFF_EP, tag + 63, lane);
      }
    } else {
      phase_d<true>(L, rs, out, tag, l, g, j, w, lane);
    }
  }
  asm volatile(".Lk_main_end:");
}

extern "C" void kernel_launch(void* const* d_in, const int* in_sizes, int n_in, void* d_out, int out_size, void* d_ws,
                              size_t ws_size, hipStream_t stream) {
  const u16* x = (const u16*)d_in[0];
  const u16* wqkv = (const u16*)d_in[1];
  const u16* wo = (const u16*)d_in[2];
  const u16* wg = (const u16*)d_in[4];
  const u16* wu = (const u16*)d_in[5];
  const u16* wd = (const u16*)d_in[6];
  unsigned char* ws = (unsigned char*)d_ws;
  (void)hipFuncSetAttribute((const void*)k_main, hipFuncAttributeMaxDynamicSharedMemorySize, MAIN_LDS);
  k_main<<<256, 256, MAIN_LDS, stream>>>(x, wqkv, wo, wg, wu, wd, ws, (u16*)d_out);
}
```
